```python
import torch

def _softmax(x, axis=-1):
    return torch.softmax(x, dim=axis)
def _gelu(x):
    return torch.nn.functional.gelu(x, approximate="tanh")
def _silu(x):
    return torch.nn.functional.silu(x)
def _sigmoid(x):
    return torch.sigmoid(x)
def _layernorm(x, axis=-1, eps=1e-5):
    m = x.mean(dim=axis, keepdim=True)
    v = x.var(dim=axis, keepdim=True, unbiased=False)
    return (x - m) / torch.sqrt(v + eps)
def _scatter_add(tgt, idx, src, axis=0):
    return tgt.clone().index_add_(axis, idx, src)
def _segment_sum(src, seg, n):
    out = torch.zeros((n,) + src.shape[1:], dtype=src.dtype)
    return out.index_add_(0, seg, src)
def _pad(x, pw):
    flat = tuple(v for pair in reversed(pw) for v in pair)
    return torch.nn.functional.pad(x, flat)

kernel_name = 'recurrent_scan__ssm_conv_s5__glu_gated_bilinear__prompt'

def setup_inputs():

    _w = (lambda a: (lambda t: t.to(torch.bfloat16) if t.is_floating_point() else t)(torch.as_tensor(a)))
    def _A_setup():
        B, S, D = 1, 2048, 448
        P = D
        Lambda_re = -0.5 * (lambda _x: torch.exp(torch.as_tensor(_x)))((lambda *s: torch.randn(*s, dtype=torch.bfloat16) * 0.8)(P))
        Lambda_im = (lambda *s: torch.randn(*s, dtype=torch.bfloat16) * 0.8)(P) * 1.0
        B_re = (lambda *s: torch.randn(*s, dtype=torch.bfloat16) * 1.6)(P, D) * (D ** -0.5)
        B_im = (lambda *s: torch.randn(*s, dtype=torch.bfloat16) * 1.6)(P, D) * (D ** -0.5)
        C_re = (lambda *s: torch.randn(*s, dtype=torch.bfloat16) * 1.6)(D, P) * (P ** -0.5)
        C_im = (lambda *s: torch.randn(*s, dtype=torch.bfloat16) * 1.6)(D, P) * (P ** -0.5)
        log_dt = (lambda *s: torch.randn(*s, dtype=torch.bfloat16) * 0.8)(P) * 0.3 - 2.0
        Dskip = (lambda *s: torch.randn(*s, dtype=torch.bfloat16) * 0.8)(D)
        return {
            "x": _w((lambda *s: torch.randn(*s, dtype=torch.bfloat16) * 0.05)(B, S, D)),
            "Lambda_re": _w(Lambda_re),
            "Lambda_im": _w(Lambda_im),
            "B_re": _w(B_re),
            "B_im": _w(B_im),
            "C_re": _w(C_re),
            "C_im": _w(C_im),
            "log_dt": _w(log_dt),
            "Dskip": _w(Dskip),
        }
    _A_in = _A_setup()

    def _B_setup():
        B, S, D, F = 1, 2048, 448, 1792
        return {
            "x": _w((lambda *s: torch.randn(*s, dtype=torch.bfloat16) * 0.05)(B, S, D)),
            "w_gate": _w((lambda *s: torch.randn(*s, dtype=torch.bfloat16) * 0.2)(D, F) * (D ** -0.5)),
            "w_up": _w((lambda *s: torch.randn(*s, dtype=torch.bfloat16) * 0.2)(D, F) * (D ** -0.5)),
            "w_down": _w((lambda *s: torch.randn(*s, dtype=torch.bfloat16) * 0.2)(F, D) * (F ** -0.5)),
        }
    _B_in = _B_setup()

    return {
        "A_x": _A_in["x"],
        "A_Lambda_re": _A_in["Lambda_re"],
        "A_Lambda_im": _A_in["Lambda_im"],
        "A_B_re": _A_in["B_re"],
        "A_B_im": _A_in["B_im"],
        "A_C_re": _A_in["C_re"],
        "A_C_im": _A_in["C_im"],
        "A_log_dt": _A_in["log_dt"],
        "A_Dskip": _A_in["Dskip"],
        "B_x": _B_in["x"],
        "B_w_gate": _B_in["w_gate"],
        "B_w_up": _B_in["w_up"],
        "B_w_down": _B_in["w_down"],
    }

def reference(A_x, A_Lambda_re, A_Lambda_im, A_B_re, A_B_im, A_C_re, A_C_im, A_log_dt, A_Dskip, B_x, B_w_gate, B_w_up, B_w_down):
    def _A_ref(x, Lambda_re, Lambda_im, B_re, B_im, C_re, C_im, log_dt, Dskip):
        Bsz, L, H = x.shape
        Lam = Lambda_re + 1j * Lambda_im
        Bmat = B_re + 1j * B_im
        Cmat = C_re + 1j * C_im
        dt = torch.exp(log_dt)
        Lam_bar = torch.exp(dt * Lam)
        B_bar = ((Lam_bar - 1.0) / Lam)[:, None] * Bmat
        Bu = torch.einsum("bld,pd->blp", x + 0j, B_bar)
        h = torch.zeros_like(Bu[:, 0, :])
        hs = []
        for t in range(L):
            h = Lam_bar[None, :] * h + Bu[:, t, :]
            hs.append(h)
        hseq = torch.stack(hs, 1)
        y = 2.0 * torch.einsum("blp,dp->bld", hseq, Cmat).real + x * Dskip[None, None, :]
        return y

    def _B_ref(x, w_gate, w_up, w_down):
        g = torch.einsum("bsd,df->bsf", x, w_gate)
        u = torch.einsum("bsd,df->bsf", x, w_up)
        h = g * u
        return torch.einsum("bsf,fd->bsd", h, w_down)

    x = A_x
    xn = _layernorm(x, axis=-1)
    s = _A_ref(xn, A_Lambda_re, A_Lambda_im, A_B_re, A_B_im, A_C_re, A_C_im, A_log_dt, A_Dskip)
    g = _silu(_B_ref(xn, B_w_gate, B_w_up, B_w_down))
    return x + s * g
```

```cpp
#include <hip/hip_runtime.h>
#include <hip/hip_bf16.h>
#include <stdint.h>
typedef unsigned short u16;
constexpr int S = 2048, D = 448, P = 448, F = 1792;
__device__ inline float b2f(u16 v) { return __uint_as_float((unsigned)v << 16); }
__device__ inline u16 f2b(float f) { unsigned u = __float_as_uint(f); return (u16)((u + 0x7FFF + ((u >> 16) & 1)) >> 16); }
__device__ inline float rb(float f) { return b2f(f2b(f)); }

__global__ void k_ln(const u16* x, u16* xn) {
    int t = blockIdx.x, l = threadIdx.x;
    float v[7]; float s = 0;
    for (int i = 0; i < 7; i++) { v[i] = b2f(x[t * D + l + 64 * i]); s += v[i]; }
    for (int o = 32; o; o >>= 1) s += __shfl_xor(s, o);
    float m = s / D, q = 0;
    for (int i = 0; i < 7; i++) { float d = v[i] - m; q += d * d; }
    for (int o = 32; o; o >>= 1) q += __shfl_xor(q, o);
    float mb = rb(m), vb = rb(q / D);
    float sd = rb(sqrtf(rb(vb + 1e-5f)));
    for (int i = 0; i < 7; i++) xn[t * D + l + 64 * i] = f2b(rb(v[i] - mb) / sd);
}
__global__ void k_bu(const u16* xn, const u16* lre, const u16* lim, const u16* bre, const u16* bim, const u16* ldt, float2* bu) {
    int t = blockIdx.x, p = threadIdx.x;
    float a = 0, b = 0;
    for (int k = 0; k < D; k++) { float xv = b2f(xn[t * D + k]); a += xv * b2f(bre[p * D + k]); b += xv * b2f(bim[p * D + k]); }
    float dt = rb(expf(b2f(ldt[p]))), lr = b2f(lre[p]), li = b2f(lim[p]);
    float e = expf(dt * lr), br = e * cosf(dt * li), bi = e * sinf(dt * li);
    float nr = br - 1.f, ni = bi, dn = lr * lr + li * li;
    float cr = (nr * lr + ni * li) / dn, ci = (ni * lr - nr * li) / dn;
    bu[t * P + p] = make_float2(cr * a - ci * b, cr * b + ci * a);
}
__global__ void k_scan(const u16* lre, const u16* lim, const u16* ldt, float2* bu) {
    int p = blockIdx.x * 64 + threadIdx.x;
    float dt = rb(expf(b2f(ldt[p]))), lr = b2f(lre[p]), li = b2f(lim[p]);
    float e = expf(dt * lr), br = e * cosf(dt * li), bi = e * sinf(dt * li);
    float hr = 0, hi = 0;
    for (int t = 0; t < S; t++) { float2 u = bu[t * P + p]; float nr = br * hr - bi * hi + u.x, ni = br * hi + bi * hr + u.y; hr = nr; hi = ni; bu[t * P + p] = make_float2(hr, hi); }
}
__global__ void k_h(const u16* xn, const u16* wg, const u16* wu, u16* h) {
    int t = blockIdx.x, f = blockIdx.y * 256 + threadIdx.x;
    float a = 0, b = 0;
    for (int k = 0; k < D; k++) { float xv = b2f(xn[t * D + k]); a += xv * b2f(wg[k * F + f]); b += xv * b2f(wu[k * F + f]); }
    h[t * F + f] = f2b(rb(a) * rb(b));
}
__global__ void k_out(const u16* x, const u16* xn, const float2* hs, const u16* cre, const u16* cim, const u16* dsk, const u16* h, const u16* wd, float* out) {
    int t = blockIdx.x, d = threadIdx.x;
    float y = 0, o = 0;
    for (int p = 0; p < P; p++) { float2 v = hs[t * P + p]; y += v.x * b2f(cre[d * P + p]) - v.y * b2f(cim[d * P + p]); }
    for (int f = 0; f < F; f++) o += b2f(h[t * F + f]) * b2f(wd[f * D + d]);
    o = rb(o);
    float g = rb(o / (1.f + expf(-o)));
    float s = 2.f * y + rb(b2f(xn[t * D + d]) * b2f(dsk[d]));
    out[t * D + d] = b2f(x[t * D + d]) + s * g;
}
extern "C" void kernel_launch(void* const* d_in, const int* in_sizes, int n_in, void* d_out, int out_size, void* d_ws, size_t ws_size, hipStream_t stream) {
    const u16* const* in = (const u16* const*)d_in;
    char* ws = (char*)d_ws;
    u16* xn = (u16*)ws; float2* bu = (float2*)(ws + (4 << 20)); u16* h = (u16*)(ws + (16 << 20));
    k_ln<<<S, 64, 0, stream>>>(in[0], xn);
    k_bu<<<S, P, 0, stream>>>(xn, in[1], in[2], in[3], in[4], in[7], bu);
    k_scan<<<P / 64, 64, 0, stream>>>(in[1], in[2], in[7], bu);
    k_h<<<dim3(S, F / 256), 256, 0, stream>>>(xn, in[10], in[11], h);
    k_out<<<S, D, 0, stream>>>(in[0], xn, bu, in[5], in[6], in[8], h, in[12], (float*)d_out);
}
```

```cpp
#include <hip/hip_runtime.h>
#include <stdint.h>
typedef unsigned short u16;
typedef unsigned int u32;
typedef unsigned long long u64;
typedef __attribute__((ext_vector_type(8))) __bf16 bf8;
typedef __attribute__((ext_vector_type(2))) __bf16 bf2;
typedef __attribute__((ext_vector_type(4))) short s16x4;
typedef __attribute__((ext_vector_type(16))) float f16v;
typedef __attribute__((ext_vector_type(4))) float f4v;
typedef __attribute__((ext_vector_type(2))) float f2v;
typedef __attribute__((ext_vector_type(4))) u32 u32x4;
typedef __attribute__((ext_vector_type(2))) u32 u32x2;
#define LDSP __attribute__((address_space(3)))
#define GLBP __attribute__((address_space(1)))

constexpr int S = 2048, D = 448, P = 448, F = 1792;
constexpr int NKAP = 168;
constexpr size_t WS_H = 0;
constexpr size_t WS_STAT = 12u << 20;
constexpr size_t WS_GRAN = WS_STAT + 65536;
constexpr size_t WS_EPOCH = WS_GRAN + 262144;

__device__ __forceinline__ float b2f(u32 lo16) { return __uint_as_float(lo16 << 16); }
__device__ __forceinline__ u32 pk(float a, float b) { bf2 v = {(__bf16)a, (__bf16)b}; return __builtin_bit_cast(u32, v); }
__device__ __forceinline__ float rb(float a) { return (float)(__bf16)a; }
__device__ __forceinline__ void dma16(const void* g, u32 lds) {
    u32 keep;
    asm volatile("s_mov_b32 %0, m0\n\ts_mov_b32 m0, %2\n\ts_nop 0\n\tglobal_load_lds_dwordx4 %1, off\n\ts_mov_b32 m0, %0" : "=&s"(keep) : "v"(g), "s"(lds) : "memory");
}
template <int N> __device__ __forceinline__ void waitvm() { asm volatile("s_waitcnt vmcnt(%0)" :: "i"(N) : "memory"); }
__device__ __forceinline__ void bar() { asm volatile("s_waitcnt lgkmcnt(0)\n\ts_barrier" ::: "memory"); }
__device__ __forceinline__ bf8 ld128(const char* smem, u32 off) { return __builtin_bit_cast(bf8, *(const u32x4*)(smem + off)); }
__device__ __forceinline__ u32x2 ldtr(const char* smem, u32 off) { return __builtin_bit_cast(u32x2, __builtin_amdgcn_ds_read_tr16_b64_v4i16((LDSP s16x4*)(smem + off))); }
__device__ __forceinline__ bf8 ldtr2(const char* smem, u32 o0, u32 o1) { u32x2 a = ldtr(smem, o0), b = ldtr(smem, o1); u32x4 v = {a.x, a.y, b.x, b.y}; return __builtin_bit_cast(bf8, v); }
__device__ __forceinline__ float swap32(float v) {
    u32 u = __float_as_uint(v); auto r = __builtin_amdgcn_permlane32_swap(u, u, false, false);
    return __uint_as_float((threadIdx.x & 32) ? r[0] : r[1]);
}
struct cf { float re, im; };
__device__ __forceinline__ cf cmul(cf a, cf b) { return {a.re * b.re - a.im * b.im, a.re * b.im + a.im * b.re}; }
__device__ __forceinline__ cf cfma(cf a, cf b, cf c) { return {fmaf(a.re, b.re, fmaf(-a.im, b.im, c.re)), fmaf(a.re, b.im, fmaf(a.im, b.re, c.im))}; }

struct K1Args { const u16 *x, *lre, *lim, *bre, *bim, *ldt, *wg, *wu; char* ws; };

constexpr int slot_of(int st) { return ((st + 7) % 10) * 16384; }

template <int ST> __device__ __forceinline__ void k1_issue(const K1Args& a, int w, int lane, int c1, int rot, u32 lbase) {
    int kb = (ST % 7) + rot; if (kb >= 7) kb -= 7;
    if constexpr (ST < 7) {
        int row = 8 * w + (lane >> 3), ch = (lane & 7) ^ ((row >> 1) & 7);
        size_t off = (size_t)(32 * c1 + row) * D + kb * 64 + ch * 8;
        dma16(a.bre + off, lbase + slot_of(ST) + w * 1024);
        dma16(a.bim + off, lbase + slot_of(ST) + 4096 + w * 1024);
    } else {
        constexpr int half = (ST - 7) / 7;
#pragma unroll
        for (int i = 0; i < 2; i++) {
            int row = 16 * w + 8 * i + (lane >> 3), ch = (lane & 7) ^ (((row >> 1) & 1) << 2);
            size_t off = (size_t)(kb * 64 + row) * F + 128 * c1 + 64 * half + ch * 8;
            dma16(a.wg + off, lbase + slot_of(ST) + (2 * w + i) * 1024);
            dma16(a.wu + off, lbase + slot_of(ST) + 8192 + (2 * w + i) * 1024);
        }
    }
}
constexpr int k1_cnt(int st) { return st < 7 ? 2 : (st < 21 ? 4 : 0); }
constexpr int k1_after(int st, int last) { int n = 0; for (int i = st + 1; i <= last && i < 21; i++) n += k1_cnt(i); return n; }

template <int HALF> __device__ __forceinline__ void ffn_pass(const K1Args& a, char* smem, u32 lbase, const bf8 (&xa)[28], int w, int lane, int r1, int c1, int rot) {
    f16v g0 = {}, g1 = {}, u0 = {}, u1 = {};
    const int h = lane >> 5, q = (lane & 15) >> 2, pp = lane & 3, blk = (lane >> 4) & 1;
    const u32 sw = ((q >> 1) & 1) << 2;
    const u32 t0 = (8 * h + q) * 128 + (((2 * blk + (pp >> 1)) ^ sw) * 16) + (pp & 1) * 8;
    const u32 t1 = t0 ^ 64;
#define STG(I) { constexpr int ST = 7 + 7 * HALF + I; \
        waitvm<k1_after(ST, ST + 8)>(); bar(); \
        if constexpr (ST + 9 < 21) k1_issue<ST + 9>(a, w, lane, c1, rot, lbase); \
        _Pragma("unroll") for (int s = 0; s < 4; s++) { \
            const u32 b = slot_of(ST) + s * 2048; \
            bf8 fg0 = ldtr2(smem, b + t0, b + t0 + 512), fg1 = ldtr2(smem, b + t1, b + t1 + 512); \
            bf8 fu0 = ldtr2(smem, b + 8192 + t0, b + 8192 + t0 + 512), fu1 = ldtr2(smem, b + 8192 + t1, b + 8192 + t1 + 512); \
            g0 = __builtin_amdgcn_mfma_f32_32x32x16_bf16(fg0, xa[4 * I + s], g0, 0, 0, 0); \
            g1 = __builtin_amdgcn_mfma_f32_32x32x16_bf16(fg1, xa[4 * I + s], g1, 0, 0, 0); \
            u0 = __builtin_amdgcn_mfma_f32_32x32x16_bf16(fu0, xa[4 * I + s], u0, 0, 0, 0); \
            u1 = __builtin_amdgcn_mfma_f32_32x32x16_bf16(fu1, xa[4 * I + s], u1, 0, 0, 0); } }
    STG(0) STG(1) STG(2) STG(3) STG(4) STG(5) STG(6)
#undef STG
    char* hb = a.ws + WS_H + ((size_t)(4 * r1 + w) * NKAP + 8 * c1 + 4 * HALF) * 1024 + lane * 16;
#pragma unroll
    for (int s = 0; s < 2; s++) {
        u32x4 v0, v1;
#pragma unroll
        for (int j = 0; j < 4; j++) {
            v0[j] = pk(rb(g0[8 * s + 2 * j]) * rb(u0[8 * s + 2 * j]), rb(g0[8 * s + 2 * j + 1]) * rb(u0[8 * s + 2 * j + 1]));
            v1[j] = pk(rb(g1[8 * s + 2 * j]) * rb(u1[8 * s + 2 * j]), rb(g1[8 * s + 2 * j + 1]) * rb(u1[8 * s + 2 * j + 1]));
        }
        *(u32x4*)(hb + s * 1024) = v0;
        *(u32x4*)(hb + (2 + s) * 1024) = v1;
    }
}

__global__ void __launch_bounds__(256, 1) k1(K1Args a) {
    __shared__ __attribute__((aligned(16))) char smem[163840];
    const u32 lbase = (u32)(size_t)(LDSP char*)smem;
    const int lane = threadIdx.x & 63, w = __builtin_amdgcn_readfirstlane(threadIdx.x >> 6);
    const int xg = blockIdx.x & 7, i = blockIdx.x >> 3;
    const int r1 = 4 * (xg >> 1) + i / 7, c1 = 7 * (xg & 1) + i % 7;
    const int rot = (c1 + 2 * (r1 & 3)) % 7;
    const int h = lane >> 5, r = lane & 31;
    const int p = 32 * c1 + r;
    const u32 plre = a.lre[p], plim = a.lim[p], pldt = a.ldt[p];
    const u32 tag = __hip_atomic_load((GLBP u32*)(a.ws + WS_EPOCH + c1 * 256), __ATOMIC_RELAXED, __HIP_MEMORY_SCOPE_AGENT) + 1;
#pragma unroll
    for (int b = 0; b < 7; b++) {
        int kb = b + rot; if (kb >= 7) kb -= 7;
#pragma unroll
        for (int p4 = 0; p4 < 4; p4++) {
            int row = 32 * w + 8 * p4 + (lane >> 3), ch = (lane & 7) ^ ((row >> 1) & 7);
            dma16(a.x + (size_t)(128 * r1 + row) * D + kb * 64 + ch * 8, lbase + b * 16384 + (4 * w + p4) * 1024);
        }
    }
    k1_issue<0>(a, w, lane, c1, rot, lbase); k1_issue<1>(a, w, lane, c1, rot, lbase); k1_issue<2>(a, w, lane, c1, rot, lbase);
    const float dt = rb(__expf(b2f(pldt))), lr = b2f(plre), li = b2f(plim);
    const float em = expf(dt * lr);
    float sn, cs; sincosf(dt * li, &sn, &cs);
    const cf lam = {em * cs, em * sn};
    const float dn = 1.f / (lr * lr + li * li);
    const cf coef = {((lam.re - 1.f) * lr + lam.im * li) * dn, (lam.im * lr - (lam.re - 1.f) * li) * dn};
    const cf l2 = cmul(lam, lam), l4 = cmul(l2, l2), l8 = cmul(l4, l4), l16 = cmul(l8, l8), l32 = cmul(l16, l16), l64 = cmul(l32, l32), l128 = cmul(l64, l64), l256 = cmul(l128, l128);
    waitvm<6>();
    bf8 xa[28];
    float s1 = 0.f, s2 = 0.f;
#pragma unroll
    for (int s = 0; s < 28; s++) {
        xa[s] = ld128(smem, (s >> 2) * 16384 + (32 * w + r) * 128 + (((2 * (s & 3) + h) ^ ((r >> 1) & 7)) * 16));
        u32x4 v = __builtin_bit_cast(u32x4, xa[s]);
#pragma unroll
        for (int j = 0; j < 4; j++) { float lo = __uint_as_float(v[j] << 16), hi = __uint_as_float(v[j] & 0xffff0000u); s1 += lo + hi; s2 = fmaf(lo, lo, fmaf(hi, hi, s2)); }
    }
    bar();
    k1_issue<3>(a, w, lane, c1, rot, lbase); k1_issue<4>(a, w, lane, c1, rot, lbase); k1_issue<5>(a, w, lane, c1, rot, lbase);
    k1_issue<6>(a, w, lane, c1, rot, lbase); k1_issue<7>(a, w, lane, c1, rot, lbase); k1_issue<8>(a, w, lane, c1, rot, lbase);
    s1 += swap32(s1); s2 += swap32(s2);
    const float mean = s1 * (1.f / D), var = fmaxf(s2 * (1.f / D) - mean * mean, 0.f);
    const float rstd = 1.f / sqrtf(var + 1e-5f), sh = -mean * rstd;
#pragma unroll
    for (int s = 0; s < 28; s++) {
        u32x4 v = __builtin_bit_cast(u32x4, xa[s]);
#pragma unroll
        for (int j = 0; j < 4; j++) v[j] = pk(fmaf(__uint_as_float(v[j] << 16), rstd, sh), fmaf(__uint_as_float(v[j] & 0xffff0000u), rstd, sh));
        xa[s] = __builtin_bit_cast(bf8, v);
    }
    f16v ar = {}, ai = {};
#define STG(ST) { waitvm<k1_after(ST, ST + 8)>(); bar(); k1_issue<ST + 9>(a, w, lane, c1, rot, lbase); \
        _Pragma("unroll") for (int s = 0; s < 4; s++) { \
            const u32 o = slot_of(ST) + r * 128 + (((2 * s + h) ^ ((r >> 1) & 7)) * 16); \
            ar = __builtin_amdgcn_mfma_f32_32x32x16_bf16(xa[4 * ST + s], ld128(smem, o), ar, 0, 0, 0); \
            ai = __builtin_amdgcn_mfma_f32_32x32x16_bf16(xa[4 * ST + s], ld128(smem, o + 4096), ai, 0, 0, 0); } }
    STG(0) STG(1) STG(2) STG(3) STG(4) STG(5) STG(6)
#undef STG
    if (c1 == 0 && h == 0) *(f2v*)(a.ws + WS_STAT + (size_t)(128 * r1 + 32 * w + r) * 8) = f2v{mean, rstd};
    cf E[8];
#pragma unroll
    for (int g = 0; g < 4; g++) {
        cf e = {ar[4 * g], ai[4 * g]};
#pragma unroll
        for (int j = 1; j < 4; j++) e = cfma(lam, e, cf{ar[4 * g + j], ai[4 * g + j]});
        cf o = {swap32(e.re), swap32(e.im)};
        E[2 * g] = h ? o : e; E[2 * g + 1] = h ? e : o;
    }
    cf agg = E[0];
#pragma unroll
    for (int g = 1; g < 8; g++) agg = cfma(l4, agg, E[g]);
    char* ex = smem + slot_of(6) + 8192;
    if (h == 0) *(f2v*)(ex + (w * 32 + r) * 8) = f2v{agg.re, agg.im};
    bar();
    cf cw = {0.f, 0.f};
#pragma unroll
    for (int v = 0; v < 3; v++) { f2v t = *(f2v*)(ex + (v * 32 + r) * 8); if (v < w) cw = cfma(l32, cw, cf{t.x, t.y}); }
    asm volatile("s_waitcnt lgkmcnt(0)" ::: "memory");
    u64* gran = (u64*)(a.ws + WS_GRAN + (size_t)c1 * 16 * 512);
    if (w == 3 && r1 < 15) {
        cf G = cfma(l32, cw, agg);
        __hip_atomic_store((GLBP u64*)(gran + (r1 * 32 + r) * 2 + h), ((u64)tag << 32) | __float_as_uint(h ? G.im : G.re), __ATOMIC_RELAXED, __HIP_MEMORY_SCOPE_AGENT);
    }
    ffn_pass<0>(a, smem, lbase, xa, w, lane, r1, c1, rot);
    ffn_pass<1>(a, smem, lbase, xa, w, lane, r1, c1, rot);
    cf cin = {0.f, 0.f};
    {
        u64 gr[8], gi[8];
        for (u32 spin = 0; spin < (1u << 20); spin++) {
            bool ok = true;
#pragma unroll
            for (int k = 0; k < 8; k++) {
                int rr = r1 - 1 - h - 2 * k; bool valid = rr >= 0; int rc = valid ? rr : 0;
                gr[k] = __hip_atomic_load((GLBP u64*)(gran + (rc * 32 + r) * 2), __ATOMIC_RELAXED, __HIP_MEMORY_SCOPE_AGENT);
                gi[k] = __hip_atomic_load((GLBP u64*)(gran + (rc * 32 + r) * 2 + 1), __ATOMIC_RELAXED, __HIP_MEMORY_SCOPE_AGENT);
                ok &= !valid || ((u32)(gr[k] >> 32) == tag && (u32)(gi[k] >> 32) == tag);
            }
            if (__all(ok)) break;
            __builtin_amdgcn_s_sleep(2);
        }
#pragma unroll
        for (int k = 7; k >= 0; k--) {
            bool valid = r1 - 1 - h - 2 * k >= 0;
            cin = cfma(l256, cin, cf{valid ? __uint_as_float((u32)gr[k]) : 0.f, valid ? __uint_as_float((u32)gi[k]) : 0.f});
        }
        if (h) cin = cmul(l128, cin);
        cin.re += swap32(cin.re); cin.im += swap32(cin.im);
    }
    if (r1 == 15 && threadIdx.x == 0) __hip_atomic_store((GLBP u32*)(a.ws + WS_EPOCH + c1 * 256), tag, __ATOMIC_RELAXED, __HIP_MEMORY_SCOPE_AGENT);
    cf lw = {1.f, 0.f};
    if (w & 1) lw = l32;
    if (w & 2) lw = cmul(lw, l64);
    cf st = cfma(lw, cin, cw);
    cf Sp[8];
#pragma unroll
    for (int g = 0; g < 8; g++) { Sp[g] = st; st = cfma(l4, st, E[g]); }
    bar();
    char* tb = smem + w * 4096;
#pragma unroll
    for (int g = 0; g < 4; g++) {
        cf e = h ? Sp[2 * g + 1] : Sp[2 * g];
        float hr[4], hi[4];
#pragma unroll
        for (int j = 0; j < 4; j++) { e = cfma(lam, e, cf{ar[4 * g + j], ai[4 * g + j]}); cf o = cmul(coef, e); hr[j] = o.re; hi[j] = -o.im; }
        u32 o = r * 64 + (((2 * g + h) ^ ((r >> 1) & 7)) * 8);
        *(u32x2*)(tb + o) = u32x2{pk(hr[0], hr[1]), pk(hr[2], hr[3])};
        *(u32x2*)(tb + 2048 + o) = u32x2{pk(hi[0], hi[1]), pk(hi[2], hi[3])};
    }
    asm volatile("s_waitcnt lgkmcnt(0)" ::: "memory");
    {
        const int q = (lane & 15) >> 2, pp = lane & 3, blk = (lane >> 4) & 1;
        char* hb = a.ws + WS_H + ((size_t)(4 * r1 + w) * NKAP + 112 + 4 * c1) * 1024 + lane * 16;
#pragma unroll
        for (int f = 0; f < 4; f++) {
            int row0 = 16 * (f & 1) + 8 * h + q, row1 = row0 + 4;
            u32 o0 = (f >> 1) * 2048 + row0 * 64 + (((4 * blk + pp) ^ ((row0 >> 1) & 7)) * 8);
            u32 o1 = (f >> 1) * 2048 + row1 * 64 + (((4 * blk + pp) ^ ((row1 >> 1) & 7)) * 8);
            *(bf8*)(hb + f * 1024) = ldtr2(tb, o0, o1);
        }
    }
}

struct K2Args { const u16 *x, *cre, *cim, *dsk, *wd; const char* ws; float* out; };

__global__ void __launch_bounds__(256, 1) k2(K2Args a) {
    __shared__ __attribute__((aligned(16))) char smem[163840];
    const u32 lbase = (u32)(size_t)(LDSP char*)smem;
    const int lane = threadIdx.x & 63, w = __builtin_amdgcn_readfirstlane(threadIdx.x >> 6);
    const int xg = blockIdx.x & 7, i = blockIdx.x >> 3;
    const int r2 = 4 * xg + i / 7, c2 = i % 7;
    const int h = lane >> 5, r = lane & 31;
    const int rot = (4 * c2 + 7 * (r2 & 3) + (xg >> 1)) % 28;
    const char* hb = a.ws + WS_H + (size_t)(2 * r2) * NKAP * 1024 + lane * 16;
    auto kap = [&](int s) { int k = s + rot; if (k >= 28) k -= 28; return 28 * w + k; };
    auto wd_issue = [&](int s) {
        int k = kap(s);
#pragma unroll
        for (int j = 0; j < 2; j++) {
            int row = 8 * j + (lane >> 3), ch = (lane & 7) ^ (((row >> 1) & 1) << 2);
            dma16(a.wd + (size_t)(16 * k + row) * D + 64 * c2 + ch * 8, lbase + w * 40960 + (s % 20) * 2048 + j * 1024);
        }
    };
    constexpr int PF = 8;
    u32x4 hb0[42], hb1[42];
    auto a_issue = [&](int s) {
        int k = s < 28 ? kap(s) : 112 + 14 * w + (s - 28);
        asm volatile("global_load_dwordx4 %0, %1, off" : "=v"(hb0[s]) : "v"(hb + (size_t)k * 1024) : "memory");
        asm volatile("global_load_dwordx4 %0, %1, off" : "=v"(hb1[s]) : "v"(hb + (size_t)(NKAP + k) * 1024) : "memory");
    };
    u32x4 cf0[14], cf1[14];
    auto c_issue = [&](int s) {
        int sg = 14 * w + s;
        const u16* cp = ((sg & 2) ? a.cim : a.cre) + (size_t)(64 * c2 + r) * P + 32 * (sg >> 2) + 16 * (sg & 1) + 8 * h;
        asm volatile("global_load_dwordx4 %0, %1, off" : "=v"(cf0[s]) : "v"(cp) : "memory");
        asm volatile("global_load_dwordx4 %0, %1, off" : "=v"(cf1[s]) : "v"(cp + 32 * P) : "memory");
    };
#pragma unroll
    for (int s = 0; s < PF; s++) { wd_issue(s); a_issue(s); }
    const int q = (lane & 15) >> 2, pp = lane & 3, blk = (lane >> 4) & 1;
    const u32 t0 = (4 * h + q) * 128 + (((2 * blk + (pp >> 1)) ^ (((q >> 1) & 1) << 2)) * 16) + (pp & 1) * 8, t1 = t0 ^ 64;
    f16v o00 = {}, o01 = {}, o10 = {}, o11 = {}, y00 = {}, y01 = {}, y10 = {}, y11 = {};
#pragma unroll
    for (int s = 0; s < 42; s++) {
        if (s + PF < 28) wd_issue(s + PF); else if (s + PF < 42) c_issue(s + PF - 28);
        if (s + PF < 42) a_issue(s + PF);
        if (s + PF < 42) waitvm<4 * PF>(); else switch (41 - s) { case 7: waitvm<28>(); break; case 6: waitvm<24>(); break; case 5: waitvm<20>(); break; case 4: waitvm<16>(); break;
            case 3: waitvm<12>(); break; case 2: waitvm<8>(); break; case 1: waitvm<4>(); break; default: waitvm<0>(); }
        asm volatile("" : "+v"(hb0[s]), "+v"(hb1[s]));
        bf8 b0 = __builtin_bit_cast(bf8, hb0[s]), b1 = __builtin_bit_cast(bf8, hb1[s]);
        if (s < 28) {
            const u32 b = w * 40960 + (s % 20) * 2048;
            bf8 w0 = ldtr2(smem, b + t0, b + t0 + 1024), w1 = ldtr2(smem, b + t1, b + t1 + 1024);
            o00 = __builtin_amdgcn_mfma_f32_32x32x16_bf16(w0, b0, o00, 0, 0, 0);
            o01 = __builtin_amdgcn_mfma_f32_32x32x16_bf16(w0, b1, o01, 0, 0, 0);
            o10 = __builtin_amdgcn_mfma_f32_32x32x16_bf16(w1, b0, o10, 0, 0, 0);
            o11 = __builtin_amdgcn_mfma_f32_32x32x16_bf16(w1, b1, o11, 0, 0, 0);
            asm volatile("s_waitcnt lgkmcnt(0)" ::: "memory");
        } else {
            asm volatile("" : "+v"(cf0[s - 28]), "+v"(cf1[s - 28]));
            bf8 w0 = __builtin_bit_cast(bf8, cf0[s - 28]), w1 = __builtin_bit_cast(bf8, cf1[s - 28]);
            y00 = __builtin_amdgcn_mfma_f32_32x32x16_bf16(w0, b0, y00, 0, 0, 0);
            y01 = __builtin_amdgcn_mfma_f32_32x32x16_bf16(w0, b1, y01, 0, 0, 0);
            y10 = __builtin_amdgcn_mfma_f32_32x32x16_bf16(w1, b0, y10, 0, 0, 0);
            y11 = __builtin_amdgcn_mfma_f32_32x32x16_bf16(w1, b1, y11, 0, 0, 0);
        }
    }
    bar();
    auto put = [&](int kind, int quad, const f16v& v) {
#pragma unroll
        for (int g = 0; g < 4; g++) *(f4v*)(smem + kind * 65536 + quad * 16384 + w * 4096 + g * 1024 + lane * 16) = f4v{v[4 * g], v[4 * g + 1], v[4 * g + 2], v[4 * g + 3]};
    };
    put(0, 0, o00); put(0, 1, o01); put(0, 2, o10); put(0, 3, o11);
    put(1, 0, y00); put(1, 1, y01); put(1, 2, y10); put(1, 3, y11);
    const int tok = 64 * r2 + 32 * (w & 1) + r, d0 = 64 * c2 + 32 * (w >> 1) + 4 * h;
    const f2v st = *(const f2v*)(a.ws + WS_STAT + (size_t)tok * 8);
    u32x2 xv[4], dv[4];
#pragma unroll
    for (int g = 0; g < 4; g++) { xv[g] = *(const u32x2*)(a.x + (size_t)tok * D + d0 + 8 * g); dv[g] = *(const u32x2*)(a.dsk + d0 + 8 * g); }
    bar();
#pragma unroll
    for (int g = 0; g < 4; g++) {
        f4v o = {}, y = {};
#pragma unroll
        for (int v = 0; v < 4; v++) { o += *(const f4v*)(smem + w * 16384 + v * 4096 + g * 1024 + lane * 16); y += *(const f4v*)(smem + 65536 + w * 16384 + v * 4096 + g * 1024 + lane * 16); }
        f4v res;
#pragma unroll
        for (int j = 0; j < 4; j++) {
            u32 xw = xv[g][j >> 1], dw = dv[g][j >> 1];
            float xf = __uint_as_float((j & 1) ? (xw & 0xffff0000u) : (xw << 16)), df = __uint_as_float((j & 1) ? (dw & 0xffff0000u) : (dw << 16));
            float xn = rb((xf - st.x) * st.y);
            float of = rb(o[j]);
            float gt = rb(of / (1.f + __expf(-of)));
            res[j] = fmaf(fmaf(2.f, y[j], rb(xn * df)), gt, xf);
        }
        *(f4v*)(a.out + (size_t)tok * D + d0 + 8 * g) = res;
    }
}

extern "C" void kernel_launch(void* const* d_in, const int* in_sizes, int n_in, void* d_out, int out_size, void* d_ws, size_t ws_size, hipStream_t stream) {
    const u16* const* in = (const u16* const*)d_in;
    K1Args a1{in[0], in[1], in[2], in[3], in[4], in[7], in[10], in[11], (char*)d_ws};
    K2Args a2{in[0], in[5], in[6], in[8], in[12], (const char*)d_ws, (float*)d_out};
    k1<<<224, 256, 0, stream>>>(a1);
    k2<<<224, 256, 0, stream>>>(a2);
}
```

```cpp
#include <hip/hip_runtime.h>
#include <stdint.h>

struct Srd { const void* p; uint32_t bytes; uint32_t flags; };
struct Args { Srd r[11]; const void* v[4]; Srd dbg; };

__global__ void __launch_bounds__(256, 1) fk(Args a) {
    __shared__ __attribute__((aligned(16))) char smem[163840];
    asm volatile("" :: "v"((__attribute__((address_space(3))) char*)smem) : "memory");
    asm volatile(R"ASM(
.Lk_start:
s_load_dwordx16 s[16:31], %0, 0x0
s_load_dwordx16 s[32:47], %0, 0x40
s_load_dwordx16 s[48:63], %0, 0x80
s_load_dwordx8 s[64:71], %0, 0xc0
s_lshr_b32 s10, %1, 3
s_and_b32 s11, %1, 7
s_mul_i32 s7, s10, 37
s_lshr_b32 s7, s7, 8
s_mul_i32 s8, s7, 7
s_sub_u32 s8, s10, s8
s_cmp_ge_u32 s10, 28
s_cbranch_scc1 .Lhelper
s_getpc_b64 s[92:93]
.Lk_pc:
s_and_b32 s91, s7, 1
s_mul_i32 s91, s91, 7
s_add_u32 s91, s91, s8
s_lshl_b32 s91, s91, 12
s_min_u32 s91, s91, .Lk_end-.Lk_pc-4096
v_lshlrev_b32 v156, 4, %2
s_add_u32 s92, s92, s91
s_addc_u32 s93, s93, 0
global_load_dwordx4 v[184:187], v156, s[92:93]
v_readfirstlane_b32 s4, %2
v_and_b32 v208, 63, %2
s_lshr_b32 s5, s11, 1
s_mov_b32 s72, s8
s_add_u32 s91, s72, 1
s_cmp_eq_u32 s91, 7
s_cselect_b32 s73, 0, s91
s_add_u32 s91, s73, 1
s_cmp_eq_u32 s91, 7
s_cselect_b32 s74, 0, s91
s_add_u32 s91, s74, 1
s_cmp_eq_u32 s91, 7
s_cselect_b32 s75, 0, s91
s_add_u32 s91, s75, 1
s_cmp_eq_u32 s91, 7
s_cselect_b32 s76, 0, s91
s_add_u32 s91, s76, 1
s_cmp_eq_u32 s91, 7
s_cselect_b32 s77, 0, s91
s_add_u32 s91, s77, 1
s_cmp_eq_u32 s91, 7
s_cselect_b32 s78, 0, s91
s_lshl_b32 s5, s5, 2
s_add_u32 s5, s5, s7
s_and_b32 s6, s11, 1
s_mul_i32 s6, s6, 7
s_add_u32 s6, s6, s8
s_lshl_b32 s11, s11, 2
s_add_u32 s7, s7, s11
s_lshr_b32 s4, s4, 6
v_lshrrev_b32 v209, 3, v208
v_and_b32 v210, 7, v208
v_lshrrev_b32 v211, 4, v208
v_and_b32 v212, 31, v208
s_lshl_b32 s10, s5, 2
s_add_u32 s10, s10, s4
s_mul_i32 s10, s10, 0x7000
v_xor_b32 v157, v210, v211
v_mul_u32_u24 v158, 0x380, v209
v_lshl_add_u32 v159, v157, 4, v158
s_lshl_b32 s11, s6, 6
s_lshl_b32 s84, s4, 11
v_add_u32 v160, s10, v159
v_lshl_add_u32 v157, v212, 1, s11
v_xor_b32 v161, 64, v160
v_add_u32 v162, 0x3000, v160
v_add_u32 v161, 0x1800, v161
s_lshl_b32 s91, s4, 12
v_add_u32 v163, 0x3000, v161
s_waitcnt lgkmcnt(0)
global_load_ushort v172, v157, s[64:65]
global_load_ushort v173, v157, s[60:61]
global_load_ushort v174, v157, s[62:63]
buffer_load_dword v175, off, s[56:59], 0 sc1
s_add_u32 m0, s91, 0
s_lshl_b32 s86, s72, 7
buffer_load_dwordx4 v160, s[16:19], s86 offen lds
v_lshrrev_b32 v213, 5, v208
s_mov_b32 s12, 0
s_mov_b32 s13, -1
s_mov_b32 s14, 0x3f803f80
s_mov_b32 s15, 0xffff0000
s_sub_u32 s24, s24, 0x400
s_subb_u32 s25, s25, 0
s_add_u32 s26, s26, 0x400
v_bfe_u32 v156, v212, 1, 3
v_xor_b32 v156, v156, v213
v_lshlrev_b32 v156, 4, v156
buffer_load_dwordx4 v161, s[16:19], s86 offen offset:1024 lds
v_lshl_add_u32 v157, v212, 7, v156
v_add_u32 v164, s91, v157
v_xor_b32 v165, 32, v164
v_xor_b32 v166, 64, v164
v_xor_b32 v167, 0x60, v164
v_add_u32 v168, 0x10000, v164
v_add_u32 v169, 0x10000, v165
v_add_u32 v170, 0x10000, v166
v_add_u32 v171, 0x10000, v167
s_lshl_b32 s10, s4, 5
buffer_load_dwordx4 v162, s[16:19], s86 offen offset:2048 lds
v_xor_b32 v13, s10, v157
v_add_u32 v14, 0x10000, v13
v_add_u32 v15, 0x20000, v13
v_and_b32 v157, 7, v212
v_lshl_add_u32 v156, v157, 7, v156
v_lshrrev_b32 v157, 3, v212
v_lshl_add_u32 v128, v157, 11, v156
v_xor_b32 v129, 32, v128
v_xor_b32 v130, 64, v128
v_xor_b32 v131, 0x60, v128
buffer_load_dwordx4 v163, s[16:19], s86 offen offset:3072 lds
v_add_u32 v132, 0x10000, v128
v_add_u32 v133, 0x10000, v129
v_add_u32 v134, 0x10000, v130
v_add_u32 v135, 0x10000, v131
v_add_u32 v136, 0x20000, v128
v_add_u32 v137, 0x20000, v129
v_add_u32 v138, 0x20000, v130
v_add_u32 v139, 0x20000, v131
v_bfe_u32 v156, v208, 2, 2
v_lshl_add_u32 v157, v213, 3, v156
s_add_u32 m0, s91, 0x4000
s_lshl_b32 s86, s73, 7
buffer_load_dwordx4 v160, s[16:19], s86 offen lds
v_lshlrev_b32 v157, 7, v157
v_bfe_u32 v156, v208, 3, 1
v_lshl_or_b32 v157, v156, 6, v157
v_bfe_u32 v156, v208, 4, 1
v_lshl_or_b32 v157, v156, 5, v157
v_and_b32 v156, 3, v208
v_lshl_or_b32 v1, v156, 3, v157
v_xor_b32 v4, 64, v1
v_add_u32 v2, 0x10000, v1
v_add_u32 v3, 0x20000, v1
buffer_load_dwordx4 v161, s[16:19], s86 offen offset:1024 lds
v_add_u32 v5, 0x10000, v4
v_add_u32 v6, 0x20000, v4
s_lshl_b32 s10, s6, 2
s_add_u32 s10, s10, s4
s_mul_i32 s10, s10, 0x1c00
s_and_b32 s11, s4, 1
s_lshl_b32 s11, s11, 6
v_xor_b32 v141, s11, v159
v_add_u32 v141, s10, v141
s_lshl_b32 s10, s8, 2
buffer_load_dwordx4 v162, s[16:19], s86 offen offset:2048 lds
s_add_u32 s10, s10, s4
s_mul_i32 s10, s10, 0x3800
v_add_u32 v9, s10, v159
v_xor_b32 v10, 64, v9
v_add_u32 v10, 0x1800, v10
v_and_b32 v156, 1, v211
v_lshlrev_b32 v156, 2, v156
v_xor_b32 v156, v156, v210
v_lshlrev_b32 v156, 4, v156
s_mul_i32 s10, s4, 0xe000
buffer_load_dwordx4 v163, s[16:19], s86 offen offset:3072 lds
s_lshl_b32 s11, s6, 8
s_add_u32 s10, s10, s11
v_mul_u32_u24 v157, 0xe00, v209
v_add3_u32 v7, v157, v156, s10
v_add_u32 v8, 0x6c00, v7
s_mul_i32 s10, s4, 0x3800
s_lshl_b32 s11, s8, 7
s_add_u32 s10, s10, s11
v_add3_u32 v11, v158, v156, s10
v_add_u32 v12, 0x1800, v11
s_add_u32 m0, s91, 0x8000
s_lshl_b32 s86, s74, 7
buffer_load_dwordx4 v160, s[16:19], s86 offen lds
s_lshl_b32 s10, s6, 13
s_add_u32 s10, s10, 0x8000
s_add_u32 s80, s56, s10
s_addc_u32 s81, s57, 0
s_mov_b32 s82, 0x2000
s_mov_b32 s83, 0x20000
s_lshl_b32 s11, s4, 2
s_sub_u32 s10, 16, s5
s_add_u32 s10, s10, s11
v_add_u32 v156, s10, v213
v_lshlrev_b32 v156, 9, v156
buffer_load_dwordx4 v161, s[16:19], s86 offen offset:1024 lds
v_lshl_add_u32 v241, v212, 4, v156
s_sub_u32 s10, 15, s5
s_lshl_b32 s10, s10, 9
s_cmp_eq_u32 s4, 3
s_cselect_b32 s10, s10, 0x40000000
v_lshlrev_b32 v156, 3, v213
v_lshl_add_u32 v156, v212, 4, v156
v_add_u32 v240, s10, v156
s_add_u32 s10, s5, 1
v_sub_u32 v156, s10, v213
v_and_b32 v156, -2, v156
buffer_load_dwordx4 v162, s[16:19], s86 offen offset:2048 lds
v_subrev_u32 v156, s11, v156
v_med3_i32 v248, v156, 0, 4
s_lshl_b32 s10, s5, 2
s_add_u32 s10, s10, s4
s_mul_i32 s11, s10, 0xa8
s_lshl_b32 s88, s6, 3
s_add_u32 s11, s11, s88
s_lshl_b32 s11, s11, 10
v_lshlrev_b32 v156, 4, v208
v_add_u32 v238, s11, v156
buffer_load_dwordx4 v163, s[16:19], s86 offen offset:3072 lds
s_lshr_b32 s11, s10, 1
s_lshl_b32 s11, s11, 5
s_and_b32 s10, s10, 1
s_mul_i32 s10, s10, 14
s_add_u32 s10, s10, s11
s_add_u32 s10, s10, s6
s_lshl_b32 s10, s10, 6
s_add_u32 s10, s10, 0x5c000
v_cmp_gt_u32 vcc, 8, v208
v_mov_b32 v156, 0x40000000
s_add_u32 m0, s91, 0xc000
s_lshl_b32 s86, s75, 7
buffer_load_dwordx4 v160, s[16:19], s86 offen lds
v_lshl_add_u32 v157, v208, 3, s10
v_cndmask_b32 v242, v156, v157, vcc
s_lshl_b32 s10, s7, 11
s_add_u32 s10, s10, 0x5c000
v_cmp_gt_u32 vcc, 28, v208
v_lshl_add_u32 v157, v208, 6, s10
s_nop 0
v_cndmask_b32 v243, v156, v157, vcc
s_lshl_b32 s10, s4, 8
s_add_u32 s10, s10, 0xa000
v_lshl_add_u32 v239, v212, 3, s10
buffer_load_dwordx4 v161, s[16:19], s86 offen offset:1024 lds
s_lshr_b32 s89, s4, 1
s_lshl_b32 s89, s89, 11
v_bfe_u32 v156, v212, 2, 1
v_xor_b32 v156, v156, v213
v_bfe_u32 v157, v212, 3, 1
v_lshlrev_b32 v157, 1, v157
v_bfe_u32 v144, v212, 1, 1
v_or_b32 v157, v157, v144
v_lshl_or_b32 v157, v156, 2, v157
v_lshrrev_b32 v156, 4, v212
buffer_load_dwordx4 v162, s[16:19], s86 offen offset:2048 lds
v_lshl_or_b32 v157, v156, 3, v157
v_and_b32 v156, 1, v212
v_lshlrev_b32 v156, 4, v156
v_mov_b32 v148, 0x3f80
v_lshlrev_b32 v156, v156, v148
v_cmp_eq_u32 vcc, 0, v157
s_nop 1
v_cndmask_b32 v144, 0, v156, vcc
v_cmp_eq_u32 vcc, 1, v157
s_nop 1
v_cndmask_b32 v145, 0, v156, vcc
v_cmp_eq_u32 vcc, 2, v157
s_nop 1
v_cndmask_b32 v146, 0, v156, vcc
v_cmp_eq_u32 vcc, 3, v157
s_nop 1
v_cndmask_b32 v147, 0, v156, vcc
v_cmp_eq_u32 vcc, 8, v157
s_nop 1
v_cndmask_b32 v148, 0, v156, vcc
buffer_load_dwordx4 v163, s[16:19], s86 offen offset:3072 lds
v_cmp_eq_u32 vcc, 9, v157
s_nop 1
v_cndmask_b32 v149, 0, v156, vcc
v_cmp_eq_u32 vcc, 10, v157
s_nop 1
v_cndmask_b32 v150, 0, v156, vcc
v_cmp_eq_u32 vcc, 11, v157
s_nop 1
v_cndmask_b32 v151, 0, v156, vcc
v_mov_b32 v152, 0
v_mov_b32 v153, 0
v_mov_b32 v154, 0
v_mov_b32 v155, 0
s_waitcnt vmcnt(12)
ds_read_b128 v[16:19], v164 offset:0
ds_read_b128 v[20:23], v165 offset:0
ds_read_b128 v[24:27], v166 offset:0
ds_read_b128 v[28:31], v167 offset:0
s_add_u32 m0, s91, 0x10000
s_lshl_b32 s86, s76, 7
buffer_load_dwordx4 v160, s[16:19], s86 offen lds
s_waitcnt lgkmcnt(3)
v_dot2c_f32_bf16 v152, s14, v16
v_dot2c_f32_bf16 v154, v16, v16
v_dot2c_f32_bf16 v153, s14, v17
v_dot2c_f32_bf16 v155, v17, v17
v_dot2c_f32_bf16 v152, s14, v18
v_dot2c_f32_bf16 v154, v18, v18
v_dot2c_f32_bf16 v153, s14, v19
v_dot2c_f32_bf16 v155, v19, v19
s_waitcnt lgkmcnt(2)
v_dot2c_f32_bf16 v152, s14, v20
v_dot2c_f32_bf16 v154, v20, v20
v_dot2c_f32_bf16 v153, s14, v21
v_dot2c_f32_bf16 v155, v21, v21
buffer_load_dwordx4 v161, s[16:19], s86 offen offset:1024 lds
v_dot2c_f32_bf16 v152, s14, v22
v_dot2c_f32_bf16 v154, v22, v22
v_dot2c_f32_bf16 v153, s14, v23
v_dot2c_f32_bf16 v155, v23, v23
s_waitcnt lgkmcnt(1)
v_dot2c_f32_bf16 v152, s14, v24
v_dot2c_f32_bf16 v154, v24, v24
v_dot2c_f32_bf16 v153, s14, v25
v_dot2c_f32_bf16 v155, v25, v25
v_dot2c_f32_bf16 v152, s14, v26
v_dot2c_f32_bf16 v154, v26, v26
v_dot2c_f32_bf16 v153, s14, v27
v_dot2c_f32_bf16 v155, v27, v27
s_waitcnt lgkmcnt(0)
v_dot2c_f32_bf16 v152, s14, v28
v_dot2c_f32_bf16 v154, v28, v28
s_waitcnt vmcnt(10)
ds_read_b128 v[32:35], v164 offset:16384
ds_read_b128 v[36:39], v165 offset:16384
ds_read_b128 v[40:43], v166 offset:16384
ds_read_b128 v[44:47], v167 offset:16384
buffer_load_dwordx4 v162, s[16:19], s86 offen offset:2048 lds
v_dot2c_f32_bf16 v153, s14, v29
v_dot2c_f32_bf16 v155, v29, v29
v_dot2c_f32_bf16 v152, s14, v30
v_dot2c_f32_bf16 v154, v30, v30
v_dot2c_f32_bf16 v153, s14, v31
v_dot2c_f32_bf16 v155, v31, v31
v_lshlrev_b32 v172, 16, v172
v_lshlrev_b32 v173, 16, v173
v_lshlrev_b32 v174, 16, v174
v_readfirstlane_b32 s9, v175
v_mul_f32 v176, 0x3fb8aa3b, v172
v_exp_f32 v176, v176
buffer_load_dwordx4 v163, s[16:19], s86 offen offset:3072 lds
v_mul_f32 v180, v173, v173
v_cvt_pk_bf16_f32 v176, v176, v176
v_lshlrev_b32 v176, 16, v176
s_add_u32 s9, s9, 1
v_mul_f32 v177, v176, v173
v_mul_f32 v178, v176, v174
v_mul_f32 v177, 0x3fb8aa3b, v177
v_mul_f32 v178, 0x3e22f983, v178
v_exp_f32 v177, v177
v_rndne_f32 v179, v178
v_sub_f32 v178, v178, v179
v_fma_f32 v180, v174, v174, v180
v_cos_f32 v179, v178
v_sin_f32 v178, v178
s_add_u32 m0, s91, 0x14000
s_lshl_b32 s86, s77, 7
buffer_load_dwordx4 v160, s[16:19], s86 offen lds
s_waitcnt lgkmcnt(3)
v_dot2c_f32_bf16 v152, s14, v32
v_dot2c_f32_bf16 v154, v32, v32
v_dot2c_f32_bf16 v153, s14, v33
v_dot2c_f32_bf16 v155, v33, v33
v_dot2c_f32_bf16 v152, s14, v34
v_dot2c_f32_bf16 v154, v34, v34
v_dot2c_f32_bf16 v153, s14, v35
v_dot2c_f32_bf16 v155, v35, v35
s_waitcnt lgkmcnt(2)
v_dot2c_f32_bf16 v152, s14, v36
v_dot2c_f32_bf16 v154, v36, v36
v_dot2c_f32_bf16 v153, s14, v37
v_dot2c_f32_bf16 v155, v37, v37
buffer_load_dwordx4 v161, s[16:19], s86 offen offset:1024 lds
v_dot2c_f32_bf16 v152, s14, v38
v_dot2c_f32_bf16 v154, v38, v38
v_dot2c_f32_bf16 v153, s14, v39
v_dot2c_f32_bf16 v155, v39, v39
s_waitcnt lgkmcnt(1)
v_dot2c_f32_bf16 v152, s14, v40
v_dot2c_f32_bf16 v154, v40, v40
v_dot2c_f32_bf16 v153, s14, v41
v_dot2c_f32_bf16 v155, v41, v41
v_dot2c_f32_bf16 v152, s14, v42
v_dot2c_f32_bf16 v154, v42, v42
v_dot2c_f32_bf16 v153, s14, v43
v_dot2c_f32_bf16 v155, v43, v43
s_waitcnt lgkmcnt(0)
v_dot2c_f32_bf16 v152, s14, v44
v_dot2c_f32_bf16 v154, v44, v44
s_waitcnt vmcnt(10)
ds_read_b128 v[48:51], v164 offset:32768
ds_read_b128 v[52:55], v165 offset:32768
ds_read_b128 v[56:59], v166 offset:32768
ds_read_b128 v[60:63], v167 offset:32768
buffer_load_dwordx4 v162, s[16:19], s86 offen offset:2048 lds
v_dot2c_f32_bf16 v153, s14, v45
v_dot2c_f32_bf16 v155, v45, v45
v_dot2c_f32_bf16 v152, s14, v46
v_dot2c_f32_bf16 v154, v46, v46
v_dot2c_f32_bf16 v153, s14, v47
v_dot2c_f32_bf16 v155, v47, v47
v_rcp_f32 v180, v180
v_mul_f32 v224, v177, v179
v_mul_f32 v225, v177, v178
v_add_f32 v181, -1.0, v224
v_mul_f32 v236, v181, v173
v_mul_f32 v237, v225, v173
buffer_load_dwordx4 v163, s[16:19], s86 offen offset:3072 lds
v_fma_f32 v236, v225, v174, v236
v_fma_f32 v237, -v181, v174, v237
v_mul_f32 v236, v236, v180
v_mul_f32 v237, v237, v180
v_mul_f32 v176, v224, v224
v_add_f32 v177, v224, v224
v_fma_f32 v176, -v225, v225, v176
v_mul_f32 v177, v177, v225
v_mul_f32 v226, v176, v176
v_add_f32 v227, v176, v176
v_fma_f32 v226, -v177, v177, v226
v_mul_f32 v227, v227, v177
v_mul_f32 v178, v226, v226
v_add_f32 v179, v226, v226
s_add_u32 m0, s91, 0x18000
s_lshl_b32 s86, s78, 7
buffer_load_dwordx4 v160, s[16:19], s86 offen lds
s_waitcnt lgkmcnt(3)
v_dot2c_f32_bf16 v152, s14, v48
v_dot2c_f32_bf16 v154, v48, v48
v_dot2c_f32_bf16 v153, s14, v49
v_dot2c_f32_bf16 v155, v49, v49
v_dot2c_f32_bf16 v152, s14, v50
v_dot2c_f32_bf16 v154, v50, v50
v_dot2c_f32_bf16 v153, s14, v51
v_dot2c_f32_bf16 v155, v51, v51
s_waitcnt lgkmcnt(2)
v_dot2c_f32_bf16 v152, s14, v52
v_dot2c_f32_bf16 v154, v52, v52
v_dot2c_f32_bf16 v153, s14, v53
v_dot2c_f32_bf16 v155, v53, v53
buffer_load_dwordx4 v161, s[16:19], s86 offen offset:1024 lds
v_dot2c_f32_bf16 v152, s14, v54
v_dot2c_f32_bf16 v154, v54, v54
v_dot2c_f32_bf16 v153, s14, v55
v_dot2c_f32_bf16 v155, v55, v55
s_waitcnt lgkmcnt(1)
v_dot2c_f32_bf16 v152, s14, v56
v_dot2c_f32_bf16 v154, v56, v56
v_dot2c_f32_bf16 v153, s14, v57
v_dot2c_f32_bf16 v155, v57, v57
v_dot2c_f32_bf16 v152, s14, v58
v_dot2c_f32_bf16 v154, v58, v58
v_dot2c_f32_bf16 v153, s14, v59
v_dot2c_f32_bf16 v155, v59, v59
s_waitcnt lgkmcnt(0)
v_dot2c_f32_bf16 v152, s14, v60
v_dot2c_f32_bf16 v154, v60, v60
s_waitcnt vmcnt(10)
ds_read_b128 v[64:67], v164 offset:49152
ds_read_b128 v[68:71], v165 offset:49152
ds_read_b128 v[72:75], v166 offset:49152
ds_read_b128 v[76:79], v167 offset:49152
buffer_load_dwordx4 v162, s[16:19], s86 offen offset:2048 lds
v_dot2c_f32_bf16 v153, s14, v61
v_dot2c_f32_bf16 v155, v61, v61
v_dot2c_f32_bf16 v152, s14, v62
v_dot2c_f32_bf16 v154, v62, v62
v_dot2c_f32_bf16 v153, s14, v63
v_dot2c_f32_bf16 v155, v63, v63
v_fma_f32 v178, -v227, v227, v178
v_mul_f32 v179, v179, v227
v_mul_f32 v176, v178, v178
v_add_f32 v177, v178, v178
v_fma_f32 v176, -v179, v179, v176
v_mul_f32 v177, v177, v179
buffer_load_dwordx4 v163, s[16:19], s86 offen offset:3072 lds
v_mul_f32 v228, v176, v176
v_add_f32 v229, v176, v176
v_fma_f32 v228, -v177, v177, v228
v_mul_f32 v229, v229, v177
v_mul_f32 v178, v228, v228
v_add_f32 v179, v228, v228
v_fma_f32 v178, -v229, v229, v178
v_mul_f32 v179, v179, v229
v_mul_f32 v176, v178, v178
v_add_f32 v177, v178, v178
v_fma_f32 v176, -v179, v179, v176
v_mul_f32 v177, v177, v179
v_mul_f32 v232, v176, v176
v_add_f32 v233, v176, v176
s_add_u32 m0, s84, 0x1c000
s_lshl_b32 s86, s72, 7
buffer_load_dwordx4 v141, s[20:23], s86 offen lds
s_waitcnt lgkmcnt(3)
v_dot2c_f32_bf16 v152, s14, v64
v_dot2c_f32_bf16 v154, v64, v64
v_dot2c_f32_bf16 v153, s14, v65
v_dot2c_f32_bf16 v155, v65, v65
v_dot2c_f32_bf16 v152, s14, v66
v_dot2c_f32_bf16 v154, v66, v66
v_dot2c_f32_bf16 v153, s14, v67
v_dot2c_f32_bf16 v155, v67, v67
s_waitcnt lgkmcnt(2)
v_dot2c_f32_bf16 v152, s14, v68
v_dot2c_f32_bf16 v154, v68, v68
v_dot2c_f32_bf16 v153, s14, v69
v_dot2c_f32_bf16 v155, v69, v69
buffer_load_dwordx4 v141, s[24:27], s86 offen offset:1024 lds
v_dot2c_f32_bf16 v152, s14, v70
v_dot2c_f32_bf16 v154, v70, v70
v_dot2c_f32_bf16 v153, s14, v71
v_dot2c_f32_bf16 v155, v71, v71
s_waitcnt lgkmcnt(1)
v_dot2c_f32_bf16 v152, s14, v72
v_dot2c_f32_bf16 v154, v72, v72
v_dot2c_f32_bf16 v153, s14, v73
v_dot2c_f32_bf16 v155, v73, v73
v_dot2c_f32_bf16 v152, s14, v74
v_dot2c_f32_bf16 v154, v74, v74
v_dot2c_f32_bf16 v153, s14, v75
v_dot2c_f32_bf16 v155, v75, v75
s_waitcnt lgkmcnt(0)
v_dot2c_f32_bf16 v152, s14, v76
v_dot2c_f32_bf16 v154, v76, v76
s_waitcnt vmcnt(10)
ds_read_b128 v[80:83], v168 offset:0
ds_read_b128 v[84:87], v169 offset:0
ds_read_b128 v[88:91], v170 offset:0
ds_read_b128 v[92:95], v171 offset:0
s_add_u32 m0, s84, 0x1e000
s_lshl_b32 s86, s73, 7
buffer_load_dwordx4 v141, s[20:23], s86 offen lds
v_dot2c_f32_bf16 v153, s14, v77
v_dot2c_f32_bf16 v155, v77, v77
v_dot2c_f32_bf16 v152, s14, v78
v_dot2c_f32_bf16 v154, v78, v78
v_dot2c_f32_bf16 v153, s14, v79
v_dot2c_f32_bf16 v155, v79, v79
v_fma_f32 v232, -v177, v177, v232
v_mul_f32 v233, v233, v177
s_bitcmp1_b32 s4, 0
s_cselect_b64 s[92:93], -1, 0
s_bitcmp1_b32 s4, 1
s_cselect_b64 s[94:95], -1, 0
v_cndmask_b32 v230, 1.0, v176, s[12:13]
v_cndmask_b32 v231, 0, v177, s[12:13]
buffer_load_dwordx4 v141, s[24:27], s86 offen offset:1024 lds
v_cndmask_b32 v234, 1.0, v228, s[92:93]
v_cndmask_b32 v235, 0, v229, s[92:93]
v_mul_f32 v180, v234, v178
v_mul_f32 v181, v234, v179
v_fma_f32 v180, -v235, v179, v180
v_fma_f32 v181, v235, v178, v181
v_cndmask_b32 v234, v234, v180, s[94:95]
v_cndmask_b32 v235, v235, v181, s[94:95]
v_mul_f32 v176, v232, v232
v_add_f32 v177, v232, v232
v_fma_f32 v176, -v233, v233, v176
v_mul_f32 v177, v177, v233
v_mul_f32 v178, v176, v176
v_add_f32 v179, v176, v176
s_add_u32 m0, s84, 0x20000
s_lshl_b32 s86, s74, 7
buffer_load_dwordx4 v141, s[20:23], s86 offen lds
s_waitcnt lgkmcnt(3)
v_dot2c_f32_bf16 v152, s14, v80
v_dot2c_f32_bf16 v154, v80, v80
v_dot2c_f32_bf16 v153, s14, v81
v_dot2c_f32_bf16 v155, v81, v81
v_dot2c_f32_bf16 v152, s14, v82
v_dot2c_f32_bf16 v154, v82, v82
v_dot2c_f32_bf16 v153, s14, v83
v_dot2c_f32_bf16 v155, v83, v83
s_waitcnt lgkmcnt(2)
v_dot2c_f32_bf16 v152, s14, v84
v_dot2c_f32_bf16 v154, v84, v84
v_dot2c_f32_bf16 v153, s14, v85
v_dot2c_f32_bf16 v155, v85, v85
buffer_load_dwordx4 v141, s[24:27], s86 offen offset:1024 lds
v_dot2c_f32_bf16 v152, s14, v86
v_dot2c_f32_bf16 v154, v86, v86
v_dot2c_f32_bf16 v153, s14, v87
v_dot2c_f32_bf16 v155, v87, v87
s_waitcnt lgkmcnt(1)
v_dot2c_f32_bf16 v152, s14, v88
v_dot2c_f32_bf16 v154, v88, v88
v_dot2c_f32_bf16 v153, s14, v89
v_dot2c_f32_bf16 v155, v89, v89
v_dot2c_f32_bf16 v152, s14, v90
v_dot2c_f32_bf16 v154, v90, v90
v_dot2c_f32_bf16 v153, s14, v91
v_dot2c_f32_bf16 v155, v91, v91
s_waitcnt lgkmcnt(0)
v_dot2c_f32_bf16 v152, s14, v92
v_dot2c_f32_bf16 v154, v92, v92
s_waitcnt vmcnt(10)
ds_read_b128 v[96:99], v168 offset:16384
ds_read_b128 v[100:103], v169 offset:16384
ds_read_b128 v[104:107], v170 offset:16384
ds_read_b128 v[108:111], v171 offset:16384
s_add_u32 m0, s84, 0x22000
s_lshl_b32 s86, s75, 7
buffer_load_dwordx4 v141, s[20:23], s86 offen lds
v_dot2c_f32_bf16 v153, s14, v93
v_dot2c_f32_bf16 v155, v93, v93
v_dot2c_f32_bf16 v152, s14, v94
v_dot2c_f32_bf16 v154, v94, v94
v_dot2c_f32_bf16 v153, s14, v95
v_dot2c_f32_bf16 v155, v95, v95
v_fma_f32 v178, -v177, v177, v178
v_mul_f32 v179, v179, v177
v_mul_f32 v180, v230, v176
v_mul_f32 v181, v230, v177
v_fma_f32 v180, -v231, v177, v180
v_fma_f32 v181, v231, v176, v181
buffer_load_dwordx4 v141, s[24:27], s86 offen offset:1024 lds
v_cndmask_b32 v230, v230, v180, s[92:93]
v_cndmask_b32 v231, v231, v181, s[92:93]
v_mul_f32 v180, v230, v178
v_mul_f32 v181, v230, v179
v_fma_f32 v180, -v231, v179, v180
v_fma_f32 v181, v231, v178, v181
v_cndmask_b32 v230, v230, v180, s[94:95]
v_cndmask_b32 v231, v231, v181, s[94:95]
v_mul_lo_u32 v248, v248, s9
v_mov_b32 v244, s9
v_cmp_gt_u32 vcc, 28, v208
v_not_b32 v245, v244
s_nop 0
v_cndmask_b32 v246, 0, v244, vcc
v_cndmask_b32 v247, 0, v245, vcc
s_add_u32 m0, s84, 0x24000
s_lshl_b32 s86, s76, 7
buffer_load_dwordx4 v141, s[20:23], s86 offen lds
s_waitcnt lgkmcnt(3)
v_dot2c_f32_bf16 v152, s14, v96
v_dot2c_f32_bf16 v154, v96, v96
v_dot2c_f32_bf16 v153, s14, v97
v_dot2c_f32_bf16 v155, v97, v97
v_dot2c_f32_bf16 v152, s14, v98
v_dot2c_f32_bf16 v154, v98, v98
v_dot2c_f32_bf16 v153, s14, v99
v_dot2c_f32_bf16 v155, v99, v99
s_waitcnt lgkmcnt(2)
v_dot2c_f32_bf16 v152, s14, v100
v_dot2c_f32_bf16 v154, v100, v100
v_dot2c_f32_bf16 v153, s14, v101
v_dot2c_f32_bf16 v155, v101, v101
buffer_load_dwordx4 v141, s[24:27], s86 offen offset:1024 lds
v_dot2c_f32_bf16 v152, s14, v102
v_dot2c_f32_bf16 v154, v102, v102
v_dot2c_f32_bf16 v153, s14, v103
v_dot2c_f32_bf16 v155, v103, v103
s_waitcnt vmcnt(10)
ds_read_b128 v[112:115], v168 offset:32768
ds_read_b128 v[116:119], v169 offset:32768
ds_read_b128 v[120:123], v170 offset:32768
ds_read_b128 v[124:127], v171 offset:32768
s_add_u32 m0, s84, 0x26000
s_lshl_b32 s86, s77, 7
buffer_load_dwordx4 v141, s[20:23], s86 offen lds
s_waitcnt lgkmcnt(5)
v_dot2c_f32_bf16 v152, s14, v104
v_dot2c_f32_bf16 v154, v104, v104
v_dot2c_f32_bf16 v153, s14, v105
v_dot2c_f32_bf16 v155, v105, v105
v_dot2c_f32_bf16 v152, s14, v106
v_dot2c_f32_bf16 v154, v106, v106
v_dot2c_f32_bf16 v153, s14, v107
v_dot2c_f32_bf16 v155, v107, v107
s_waitcnt lgkmcnt(4)
v_dot2c_f32_bf16 v152, s14, v108
v_dot2c_f32_bf16 v154, v108, v108
v_dot2c_f32_bf16 v153, s14, v109
v_dot2c_f32_bf16 v155, v109, v109
buffer_load_dwordx4 v141, s[24:27], s86 offen offset:1024 lds
v_dot2c_f32_bf16 v152, s14, v110
v_dot2c_f32_bf16 v154, v110, v110
v_dot2c_f32_bf16 v153, s14, v111
v_dot2c_f32_bf16 v155, v111, v111
s_waitcnt lgkmcnt(3)
v_dot2c_f32_bf16 v152, s14, v112
v_dot2c_f32_bf16 v154, v112, v112
v_dot2c_f32_bf16 v153, s14, v113
v_dot2c_f32_bf16 v155, v113, v113
v_dot2c_f32_bf16 v152, s14, v114
v_dot2c_f32_bf16 v154, v114, v114
v_dot2c_f32_bf16 v153, s14, v115
v_dot2c_f32_bf16 v155, v115, v115
s_waitcnt lgkmcnt(2)
v_dot2c_f32_bf16 v152, s14, v116
v_dot2c_f32_bf16 v154, v116, v116
v_dot2c_f32_bf16 v153, s14, v117
v_dot2c_f32_bf16 v155, v117, v117
v_dot2c_f32_bf16 v152, s14, v118
v_dot2c_f32_bf16 v154, v118, v118
v_dot2c_f32_bf16 v153, s14, v119
v_dot2c_f32_bf16 v155, v119, v119
s_waitcnt lgkmcnt(1)
v_dot2c_f32_bf16 v152, s14, v120
v_dot2c_f32_bf16 v154, v120, v120
v_dot2c_f32_bf16 v153, s14, v121
v_dot2c_f32_bf16 v155, v121, v121
v_dot2c_f32_bf16 v152, s14, v122
v_dot2c_f32_bf16 v154, v122, v122
v_dot2c_f32_bf16 v153, s14, v123
v_dot2c_f32_bf16 v155, v123, v123
s_waitcnt lgkmcnt(0)
v_dot2c_f32_bf16 v152, s14, v124
v_dot2c_f32_bf16 v154, v124, v124
v_dot2c_f32_bf16 v153, s14, v125
v_dot2c_f32_bf16 v155, v125, v125
v_dot2c_f32_bf16 v152, s14, v126
v_dot2c_f32_bf16 v154, v126, v126
v_dot2c_f32_bf16 v153, s14, v127
v_dot2c_f32_bf16 v155, v127, v127
s_nop 1
v_add_f32 v152, v152, v153
v_add_f32 v154, v154, v155
v_mov_b32 v153, v152
v_mov_b32 v155, v154
s_nop 0
v_permlane32_swap_b32 v152, v153
v_permlane32_swap_b32 v154, v155
v_add_f32 v152, v152, v153
v_add_f32 v154, v154, v155
v_mul_f32 v152, 0x3b124925, v152
v_mul_f32 v154, 0x3b124925, v154
v_fma_f32 v154, -v152, v152, v154
v_max_f32 v154, 0, v154
v_cvt_pk_bf16_f32 v152, v152, v154
v_and_b32 v154, s15, v152
v_lshlrev_b32 v152, 16, v152
v_xor_b32 v192, 0x80000000, v152
v_add_f32 v154, 0x3727c5ac, v154
v_mov_b32 v193, v192
v_mov_b64 v[194:195], v[192:193]
v_mov_b64 v[196:197], v[192:193]
v_mov_b64 v[198:199], v[192:193]
v_mov_b64 v[200:201], v[192:193]
v_mov_b64 v[202:203], v[192:193]
v_mov_b64 v[204:205], v[192:193]
v_mov_b64 v[206:207], v[192:193]
s_nop 1
v_mfma_f32_32x32x16_bf16 v[160:175], v[144:147], v[16:19], v[192:207]
v_cvt_pk_bf16_f32 v154, v154, v154
v_and_b32 v154, s15, v154
v_sqrt_f32 v154, v154
v_mfma_f32_32x32x16_bf16 v[160:175], v[148:151], v[20:23], v[160:175]
s_lshl_b32 s10, s5, 2
v_cvt_pk_bf16_f32 v154, v154, v154
v_and_b32 v154, s15, v154
v_rcp_f32 v153, v154
v_mfma_f32_32x32x16_bf16 v[176:191], v[144:147], v[24:27], v[192:207]
s_waitcnt vmcnt(10)
s_barrier
ds_read_b128 a[208:211], v132 offset:49152
ds_read_b128 a[212:215], v132 offset:50176
v_mov_b32 v140, v153
v_mfma_f32_32x32x16_bf16 v[176:191], v[148:151], v[28:31], v[176:191]
s_add_u32 s10, s10, s4
s_lshl_b32 s10, s10, 8
s_lshl_b32 s11, s6, 14
s_add_u32 s10, s10, s11
s_add_u32 s10, s10, 0x24000
v_lshlrev_b32 v156, 3, v212
v_mul_f32 v160, v140, v160
v_mul_f32 v161, v140, v161
v_mul_f32 v162, v140, v162
v_mul_f32 v163, v140, v163
v_cvt_pk_bf16_f32 v16, v160, v161
v_cvt_pk_bf16_f32 v17, v162, v163
v_mul_f32 v164, v140, v164
v_mul_f32 v165, v140, v165
v_mul_f32 v166, v140, v166
v_mul_f32 v167, v140, v167
v_cvt_pk_bf16_f32 v18, v164, v165
v_cvt_pk_bf16_f32 v19, v166, v167
v_mul_f32 v168, v140, v168
v_mul_f32 v169, v140, v169
v_mul_f32 v170, v140, v170
v_mul_f32 v171, v140, v171
v_cvt_pk_bf16_f32 v20, v168, v169
v_cvt_pk_bf16_f32 v21, v170, v171
v_mul_f32 v172, v140, v172
v_mul_f32 v173, v140, v173
v_mul_f32 v174, v140, v174
v_mul_f32 v175, v140, v175
v_cvt_pk_bf16_f32 v22, v172, v173
v_cvt_pk_bf16_f32 v23, v174, v175
buffer_store_dwordx2 v[152:153], v156, s[56:59], s10 offen sc1
s_waitcnt lgkmcnt(0)
v_mfma_f32_32x32x16_bf16 a[112:127], v[16:19], a[208:211], 0
ds_read_b128 a[224:227], v133 offset:49152
ds_read_b128 a[228:231], v133 offset:50176
v_mul_f32 v176, v140, v176
v_mul_f32 v177, v140, v177
v_mul_f32 v178, v140, v178
s_add_u32 m0, s84, 0
s_mov_b32 s87, 0
buffer_load_dwordx4 v9, s[36:39], s87 offen lds
v_mfma_f32_32x32x16_bf16 a[128:143], v[16:19], a[212:215], 0
v_mul_f32 v179, v140, v179
v_cvt_pk_bf16_f32 v24, v176, v177
v_cvt_pk_bf16_f32 v25, v178, v179
v_mul_f32 v180, v140, v180
v_mul_f32 v181, v140, v181
buffer_load_dwordx4 v10, s[36:39], s87 offen offset:1024 lds
v_mfma_f32_32x32x16_bf16 v[160:175], v[144:147], v[32:35], v[192:207]
v_mul_f32 v182, v140, v182
v_mul_f32 v183, v140, v183
v_cvt_pk_bf16_f32 v26, v180, v181
v_cvt_pk_bf16_f32 v27, v182, v183
s_waitcnt lgkmcnt(0)
v_mfma_f32_32x32x16_bf16 a[112:127], v[20:23], a[224:227], a[112:127]
ds_read_b128 a[208:211], v134 offset:49152
ds_read_b128 a[212:215], v134 offset:50176
v_mul_f32 v184, v140, v184
v_mul_f32 v185, v140, v185
v_mul_f32 v186, v140, v186
s_add_u32 m0, s84, 0x2000
s_mov_b32 s87, 0x80
buffer_load_dwordx4 v9, s[36:39], s87 offen lds
v_mfma_f32_32x32x16_bf16 a[128:143], v[20:23], a[228:231], a[128:143]
v_mul_f32 v187, v140, v187
v_cvt_pk_bf16_f32 v28, v184, v185
v_cvt_pk_bf16_f32 v29, v186, v187
v_mul_f32 v188, v140, v188
v_mul_f32 v189, v140, v189
buffer_load_dwordx4 v10, s[36:39], s87 offen offset:1024 lds
v_mfma_f32_32x32x16_bf16 v[160:175], v[148:151], v[36:39], v[160:175]
v_mul_f32 v190, v140, v190
v_mul_f32 v191, v140, v191
v_cvt_pk_bf16_f32 v30, v188, v189
v_cvt_pk_bf16_f32 v31, v190, v191
s_waitcnt lgkmcnt(0)
v_mfma_f32_32x32x16_bf16 a[112:127], v[24:27], a[208:211], a[112:127]
ds_read_b128 a[224:227], v135 offset:49152
ds_read_b128 a[228:231], v135 offset:50176
v_mfma_f32_32x32x16_bf16 a[128:143], v[24:27], a[212:215], a[128:143]
s_nop 2
v_mul_f32 v160, v140, v160
v_mul_f32 v161, v140, v161
v_mul_f32 v162, v140, v162
v_mul_f32 v163, v140, v163
v_cvt_pk_bf16_f32 v32, v160, v161
v_cvt_pk_bf16_f32 v33, v162, v163
v_mul_f32 v164, v140, v164
s_add_u32 m0, s84, 0x4000
s_mov_b32 s87, 0x100
buffer_load_dwordx4 v9, s[36:39], s87 offen lds
v_mfma_f32_32x32x16_bf16 v[176:191], v[144:147], v[40:43], v[192:207]
v_mul_f32 v165, v140, v165
v_mul_f32 v166, v140, v166
v_mul_f32 v167, v140, v167
v_cvt_pk_bf16_f32 v34, v164, v165
v_cvt_pk_bf16_f32 v35, v166, v167
buffer_load_dwordx4 v10, s[36:39], s87 offen offset:1024 lds
s_waitcnt lgkmcnt(0)
v_mfma_f32_32x32x16_bf16 a[112:127], v[28:31], a[224:227], a[112:127]
s_waitcnt vmcnt(15)
s_barrier
ds_read_b128 a[208:211], v132 offset:57344
ds_read_b128 a[212:215], v132 offset:58368
v_mul_f32 v168, v140, v168
v_mfma_f32_32x32x16_bf16 a[128:143], v[28:31], a[228:231], a[128:143]
v_mul_f32 v169, v140, v169
v_mul_f32 v170, v140, v170
v_mul_f32 v171, v140, v171
v_cvt_pk_bf16_f32 v36, v168, v169
v_cvt_pk_bf16_f32 v37, v170, v171
v_mul_f32 v172, v140, v172
v_mfma_f32_32x32x16_bf16 v[176:191], v[148:151], v[44:47], v[176:191]
v_mul_f32 v173, v140, v173
v_mul_f32 v174, v140, v174
v_mul_f32 v175, v140, v175
v_cvt_pk_bf16_f32 v38, v172, v173
v_cvt_pk_bf16_f32 v39, v174, v175
s_add_u32 m0, s84, 0x6000
s_mov_b32 s87, 0x180
buffer_load_dwordx4 v9, s[36:39], s87 offen lds
s_waitcnt lgkmcnt(0)
v_mfma_f32_32x32x16_bf16 a[112:127], v[32:35], a[208:211], a[112:127]
ds_read_b128 a[224:227], v133 offset:57344
ds_read_b128 a[228:231], v133 offset:58368
buffer_load_dwordx4 v10, s[36:39], s87 offen offset:1024 lds
v_mfma_f32_32x32x16_bf16 a[128:143], v[32:35], a[212:215], a[128:143]
v_mul_f32 v176, v140, v176
v_mul_f32 v177, v140, v177
v_mul_f32 v178, v140, v178
v_mul_f32 v179, v140, v179
v_cvt_pk_bf16_f32 v40, v176, v177
v_cvt_pk_bf16_f32 v41, v178, v179
v_mfma_f32_32x32x16_bf16 v[160:175], v[144:147], v[48:51], v[192:207]
v_mul_f32 v180, v140, v180
v_mul_f32 v181, v140, v181
v_mul_f32 v182, v140, v182
v_mul_f32 v183, v140, v183
v_cvt_pk_bf16_f32 v42, v180, v181
s_waitcnt lgkmcnt(0)
v_mfma_f32_32x32x16_bf16 a[112:127], v[36:39], a[224:227], a[112:127]
ds_read_b128 a[208:211], v134 offset:57344
ds_read_b128 a[212:215], v134 offset:58368
v_cvt_pk_bf16_f32 v43, v182, v183
v_mul_f32 v184, v140, v184
v_mul_f32 v185, v140, v185
s_add_u32 m0, s84, 0x8000
s_lshl_b32 s86, s78, 7
buffer_load_dwordx4 v141, s[20:23], s86 offen lds
v_mfma_f32_32x32x16_bf16 a[128:143], v[36:39], a[228:231], a[128:143]
v_mul_f32 v186, v140, v186
v_mul_f32 v187, v140, v187
v_cvt_pk_bf16_f32 v44, v184, v185
v_cvt_pk_bf16_f32 v45, v186, v187
v_mul_f32 v188, v140, v188
v_mul_f32 v189, v140, v189
buffer_load_dwordx4 v141, s[24:27], s86 offen offset:1024 lds
v_mfma_f32_32x32x16_bf16 v[160:175], v[148:151], v[52:55], v[160:175]
v_mul_f32 v190, v140, v190
v_mul_f32 v191, v140, v191
v_cvt_pk_bf16_f32 v46, v188, v189
v_cvt_pk_bf16_f32 v47, v190, v191
s_waitcnt lgkmcnt(0)
v_mfma_f32_32x32x16_bf16 a[112:127], v[40:43], a[208:211], a[112:127]
ds_read_b128 a[224:227], v135 offset:57344
ds_read_b128 a[228:231], v135 offset:58368
v_mfma_f32_32x32x16_bf16 a[128:143], v[40:43], a[212:215], a[128:143]
s_nop 2
v_mul_f32 v160, v140, v160
v_mul_f32 v161, v140, v161
v_mul_f32 v162, v140, v162
v_mul_f32 v163, v140, v163
v_cvt_pk_bf16_f32 v48, v160, v161
v_cvt_pk_bf16_f32 v49, v162, v163
v_mul_f32 v164, v140, v164
s_add_u32 m0, s84, 0xa000
s_mov_b32 s87, 0x200
buffer_load_dwordx4 v9, s[36:39], s87 offen lds
v_mfma_f32_32x32x16_bf16 v[176:191], v[144:147], v[56:59], v[192:207]
v_mul_f32 v165, v140, v165
v_mul_f32 v166, v140, v166
v_mul_f32 v167, v140, v167
v_cvt_pk_bf16_f32 v50, v164, v165
v_cvt_pk_bf16_f32 v51, v166, v167
buffer_load_dwordx4 v10, s[36:39], s87 offen offset:1024 lds
s_waitcnt lgkmcnt(0)
v_mfma_f32_32x32x16_bf16 a[112:127], v[44:47], a[224:227], a[112:127]
s_waitcnt vmcnt(10)
s_barrier
ds_read_b128 a[208:211], v136 offset:0
ds_read_b128 a[212:215], v136 offset:1024
v_mul_f32 v168, v140, v168
v_mfma_f32_32x32x16_bf16 a[128:143], v[44:47], a[228:231], a[128:143]
v_mul_f32 v169, v140, v169
v_mul_f32 v170, v140, v170
v_mul_f32 v171, v140, v171
v_cvt_pk_bf16_f32 v52, v168, v169
v_cvt_pk_bf16_f32 v53, v170, v171
v_mul_f32 v172, v140, v172
s_add_u32 m0, s84, 0xc000
s_mul_i32 s85, s72, 0x38000
buffer_load_dwordx4 v7, s[28:31], s85 offen lds
v_mfma_f32_32x32x16_bf16 v[176:191], v[148:151], v[60:63], v[176:191]
v_mul_f32 v173, v140, v173
v_mul_f32 v174, v140, v174
v_mul_f32 v175, v140, v175
v_cvt_pk_bf16_f32 v54, v172, v173
v_cvt_pk_bf16_f32 v55, v174, v175
s_waitcnt lgkmcnt(0)
v_mfma_f32_32x32x16_bf16 a[112:127], v[48:51], a[208:211], a[112:127]
ds_read_b128 a[224:227], v137 offset:0
ds_read_b128 a[228:231], v137 offset:1024
buffer_load_dwordx4 v8, s[28:31], s85 offen offset:1024 lds
v_mfma_f32_32x32x16_bf16 a[128:143], v[48:51], a[212:215], a[128:143]
s_nop 0
v_mul_f32 v176, v140, v176
v_mul_f32 v177, v140, v177
v_mul_f32 v178, v140, v178
v_mul_f32 v179, v140, v179
v_cvt_pk_bf16_f32 v56, v176, v177
v_cvt_pk_bf16_f32 v57, v178, v179
v_mul_f32 v180, v140, v180
v_mfma_f32_32x32x16_bf16 v[160:175], v[144:147], v[64:67], v[192:207]
v_mul_f32 v181, v140, v181
v_mul_f32 v182, v140, v182
v_mul_f32 v183, v140, v183
v_cvt_pk_bf16_f32 v58, v180, v181
v_cvt_pk_bf16_f32 v59, v182, v183
s_add_u32 m0, s84, 0xe000
s_nop 0
buffer_load_dwordx4 v7, s[32:35], s85 offen lds
s_waitcnt lgkmcnt(0)
v_mfma_f32_32x32x16_bf16 a[112:127], v[52:55], a[224:227], a[112:127]
ds_read_b128 a[208:211], v138 offset:0
ds_read_b128 a[212:215], v138 offset:1024
v_mul_f32 v184, v140, v184
v_mul_f32 v185, v140, v185
v_mul_f32 v186, v140, v186
v_mfma_f32_32x32x16_bf16 a[128:143], v[52:55], a[228:231], a[128:143]
ds_read_b128 a[0:3], v13 offset:0
v_mul_f32 v187, v140, v187
v_cvt_pk_bf16_f32 v60, v184, v185
v_cvt_pk_bf16_f32 v61, v186, v187
v_mul_f32 v188, v140, v188
v_mul_f32 v189, v140, v189
buffer_load_dwordx4 v8, s[32:35], s85 offen offset:1024 lds
v_mfma_f32_32x32x16_bf16 v[160:175], v[148:151], v[68:71], v[160:175]
ds_read_b128 a[4:7], v13 offset:4096
v_mul_f32 v190, v140, v190
v_mul_f32 v191, v140, v191
v_cvt_pk_bf16_f32 v62, v188, v189
v_cvt_pk_bf16_f32 v63, v190, v191
s_waitcnt lgkmcnt(2)
v_mfma_f32_32x32x16_bf16 a[112:127], v[56:59], a[208:211], a[112:127]
ds_read_b128 a[224:227], v139 offset:0
ds_read_b128 a[228:231], v139 offset:1024
s_add_u32 m0, s84, 0x10000
s_mov_b32 s87, 0x280
buffer_load_dwordx4 v9, s[36:39], s87 offen lds
v_mfma_f32_32x32x16_bf16 a[128:143], v[56:59], a[212:215], a[128:143]
v_mul_f32 v160, v140, v160
v_mul_f32 v161, v140, v161
v_mul_f32 v162, v140, v162
v_mul_f32 v163, v140, v163
v_cvt_pk_bf16_f32 v64, v160, v161
v_cvt_pk_bf16_f32 v65, v162, v163
v_mul_f32 v164, v140, v164
v_mfma_f32_32x32x16_bf16 v[176:191], v[144:147], v[72:75], v[192:207]
v_mul_f32 v165, v140, v165
v_mul_f32 v166, v140, v166
v_mul_f32 v167, v140, v167
v_cvt_pk_bf16_f32 v66, v164, v165
v_cvt_pk_bf16_f32 v67, v166, v167
buffer_load_dwordx4 v10, s[36:39], s87 offen offset:1024 lds
s_waitcnt lgkmcnt(0)
v_mfma_f32_32x32x16_bf16 a[112:127], v[60:63], a[224:227], a[112:127]
s_waitcnt vmcnt(14)
s_barrier
ds_read_b128 a[208:211], v136 offset:8192
ds_read_b128 a[212:215], v136 offset:9216
v_mul_f32 v168, v140, v168
v_mfma_f32_32x32x16_bf16 a[128:143], v[60:63], a[228:231], a[128:143]
v_mul_f32 v169, v140, v169
v_mul_f32 v170, v140, v170
v_mul_f32 v171, v140, v171
v_cvt_pk_bf16_f32 v68, v168, v169
v_cvt_pk_bf16_f32 v69, v170, v171
v_mul_f32 v172, v140, v172
s_add_u32 m0, s84, 0x12000
s_mul_i32 s85, s73, 0x38000
buffer_load_dwordx4 v7, s[28:31], s85 offen lds
v_mfma_f32_32x32x16_bf16 v[176:191], v[148:151], v[76:79], v[176:191]
v_mul_f32 v173, v140, v173
v_mul_f32 v174, v140, v174
v_mul_f32 v175, v140, v175
v_cvt_pk_bf16_f32 v70, v172, v173
v_cvt_pk_bf16_f32 v71, v174, v175
buffer_load_dwordx4 v8, s[28:31], s85 offen offset:1024 lds
s_waitcnt lgkmcnt(0)
v_mfma_f32_32x32x16_bf16 a[112:127], v[64:67], a[208:211], a[112:127]
ds_read_b128 a[224:227], v137 offset:8192
ds_read_b128 a[228:231], v137 offset:9216
v_mfma_f32_32x32x16_bf16 a[128:143], v[64:67], a[212:215], a[128:143]
s_nop 0
v_mul_f32 v176, v140, v176
v_mul_f32 v177, v140, v177
v_mul_f32 v178, v140, v178
v_mul_f32 v179, v140, v179
v_cvt_pk_bf16_f32 v72, v176, v177
v_cvt_pk_bf16_f32 v73, v178, v179
v_mul_f32 v180, v140, v180
s_add_u32 m0, s84, 0x14000
s_nop 0
buffer_load_dwordx4 v7, s[32:35], s85 offen lds
v_mfma_f32_32x32x16_bf16 v[160:175], v[144:147], v[80:83], v[192:207]
v_mul_f32 v181, v140, v181
v_mul_f32 v182, v140, v182
v_mul_f32 v183, v140, v183
v_cvt_pk_bf16_f32 v74, v180, v181
v_cvt_pk_bf16_f32 v75, v182, v183
buffer_load_dwordx4 v8, s[32:35], s85 offen offset:1024 lds
s_waitcnt lgkmcnt(0)
v_mfma_f32_32x32x16_bf16 a[112:127], v[68:71], a[224:227], a[112:127]
ds_read_b128 a[208:211], v138 offset:8192
ds_read_b128 a[212:215], v138 offset:9216
v_mul_f32 v184, v140, v184
v_mul_f32 v185, v140, v185
v_mul_f32 v186, v140, v186
v_mfma_f32_32x32x16_bf16 a[128:143], v[68:71], a[228:231], a[128:143]
ds_read_b128 a[8:11], v13 offset:8192
v_mul_f32 v187, v140, v187
v_cvt_pk_bf16_f32 v76, v184, v185
v_cvt_pk_bf16_f32 v77, v186, v187
v_mul_f32 v188, v140, v188
v_mul_f32 v189, v140, v189
v_mfma_f32_32x32x16_bf16 v[160:175], v[148:151], v[84:87], v[160:175]
ds_read_b128 a[12:15], v13 offset:12288
v_mul_f32 v190, v140, v190
v_mul_f32 v191, v140, v191
v_cvt_pk_bf16_f32 v78, v188, v189
v_cvt_pk_bf16_f32 v79, v190, v191
s_add_u32 m0, s84, 0x16000
s_mov_b32 s87, 0x300
buffer_load_dwordx4 v9, s[36:39], s87 offen lds
s_waitcnt lgkmcnt(2)
v_mfma_f32_32x32x16_bf16 a[112:127], v[72:75], a[208:211], a[112:127]
ds_read_b128 a[224:227], v139 offset:8192
ds_read_b128 a[228:231], v139 offset:9216
buffer_load_dwordx4 v10, s[36:39], s87 offen offset:1024 lds
v_mfma_f32_32x32x16_bf16 a[128:143], v[72:75], a[212:215], a[128:143]
v_mul_f32 v160, v140, v160
v_mul_f32 v161, v140, v161
v_mul_f32 v162, v140, v162
v_mul_f32 v163, v140, v163
v_cvt_pk_bf16_f32 v80, v160, v161
v_cvt_pk_bf16_f32 v81, v162, v163
v_mul_f32 v164, v140, v164
v_mfma_f32_32x32x16_bf16 v[176:191], v[144:147], v[88:91], v[192:207]
v_mul_f32 v165, v140, v165
v_mul_f32 v166, v140, v166
v_mul_f32 v167, v140, v167
v_cvt_pk_bf16_f32 v82, v164, v165
v_cvt_pk_bf16_f32 v83, v166, v167
s_waitcnt lgkmcnt(0)
v_mfma_f32_32x32x16_bf16 a[112:127], v[76:79], a[224:227], a[112:127]
s_waitcnt vmcnt(18)
s_barrier
ds_read_b128 a[208:211], v136 offset:16384
ds_read_b128 a[212:215], v136 offset:17408
v_mul_f32 v168, v140, v168
s_add_u32 m0, s84, 0x18000
s_mul_i32 s85, s74, 0x38000
buffer_load_dwordx4 v7, s[28:31], s85 offen lds
v_mfma_f32_32x32x16_bf16 a[128:143], v[76:79], a[228:231], a[128:143]
v_mul_f32 v169, v140, v169
v_mul_f32 v170, v140, v170
v_mul_f32 v171, v140, v171
v_cvt_pk_bf16_f32 v84, v168, v169
v_cvt_pk_bf16_f32 v85, v170, v171
v_mul_f32 v172, v140, v172
buffer_load_dwordx4 v8, s[28:31], s85 offen offset:1024 lds
v_mfma_f32_32x32x16_bf16 v[176:191], v[148:151], v[92:95], v[176:191]
v_mul_f32 v173, v140, v173
v_mul_f32 v174, v140, v174
v_mul_f32 v175, v140, v175
v_cvt_pk_bf16_f32 v86, v172, v173
v_cvt_pk_bf16_f32 v87, v174, v175
s_waitcnt lgkmcnt(0)
v_mfma_f32_32x32x16_bf16 a[112:127], v[80:83], a[208:211], a[112:127]
ds_read_b128 a[224:227], v137 offset:16384
ds_read_b128 a[228:231], v137 offset:17408
s_add_u32 m0, s84, 0x1a000
s_nop 0
buffer_load_dwordx4 v7, s[32:35], s85 offen lds
v_mfma_f32_32x32x16_bf16 a[128:143], v[80:83], a[212:215], a[128:143]
v_mul_f32 v176, v140, v176
v_mul_f32 v177, v140, v177
v_mul_f32 v178, v140, v178
v_mul_f32 v179, v140, v179
v_cvt_pk_bf16_f32 v88, v176, v177
v_cvt_pk_bf16_f32 v89, v178, v179
v_mul_f32 v180, v140, v180
v_mfma_f32_32x32x16_bf16 v[160:175], v[144:147], v[96:99], v[192:207]
v_mul_f32 v181, v140, v181
v_mul_f32 v182, v140, v182
v_mul_f32 v183, v140, v183
v_cvt_pk_bf16_f32 v90, v180, v181
v_cvt_pk_bf16_f32 v91, v182, v183
buffer_load_dwordx4 v8, s[32:35], s85 offen offset:1024 lds
s_waitcnt lgkmcnt(0)
v_mfma_f32_32x32x16_bf16 a[112:127], v[84:87], a[224:227], a[112:127]
ds_read_b128 a[208:211], v138 offset:16384
ds_read_b128 a[212:215], v138 offset:17408
v_mul_f32 v184, v140, v184
v_mul_f32 v185, v140, v185
v_mul_f32 v186, v140, v186
v_mfma_f32_32x32x16_bf16 a[128:143], v[84:87], a[228:231], a[128:143]
ds_read_b128 a[16:19], v13 offset:16384
v_mul_f32 v187, v140, v187
v_cvt_pk_bf16_f32 v92, v184, v185
v_cvt_pk_bf16_f32 v93, v186, v187
v_mul_f32 v188, v140, v188
v_mul_f32 v189, v140, v189
s_add_u32 m0, s84, 0x1c000
s_mov_b32 s87, 0
buffer_load_dwordx4 v9, s[40:43], s87 offen lds
v_mfma_f32_32x32x16_bf16 v[160:175], v[148:151], v[100:103], v[160:175]
ds_read_b128 a[20:23], v13 offset:20480
v_mul_f32 v190, v140, v190
v_mul_f32 v191, v140, v191
v_cvt_pk_bf16_f32 v94, v188, v189
v_cvt_pk_bf16_f32 v95, v190, v191
s_waitcnt lgkmcnt(2)
v_mfma_f32_32x32x16_bf16 a[112:127], v[88:91], a[208:211], a[112:127]
ds_read_b128 a[224:227], v139 offset:16384
ds_read_b128 a[228:231], v139 offset:17408
buffer_load_dwordx4 v10, s[40:43], s87 offen offset:1024 lds
v_mfma_f32_32x32x16_bf16 a[128:143], v[88:91], a[212:215], a[128:143]
s_nop 0
v_mul_f32 v160, v140, v160
v_mul_f32 v161, v140, v161
v_mul_f32 v162, v140, v162
v_mul_f32 v163, v140, v163
v_cvt_pk_bf16_f32 v96, v160, v161
v_cvt_pk_bf16_f32 v97, v162, v163
v_mul_f32 v164, v140, v164
v_mfma_f32_32x32x16_bf16 v[176:191], v[144:147], v[104:107], v[192:207]
v_mul_f32 v165, v140, v165
v_mul_f32 v166, v140, v166
v_mul_f32 v167, v140, v167
v_cvt_pk_bf16_f32 v98, v164, v165
v_cvt_pk_bf16_f32 v99, v166, v167
s_add_u32 m0, s84, 0x1e000
s_mul_i32 s85, s75, 0x38000
buffer_load_dwordx4 v7, s[28:31], s85 offen lds
s_waitcnt lgkmcnt(0)
v_mfma_f32_32x32x16_bf16 a[112:127], v[92:95], a[224:227], a[112:127]
s_waitcnt vmcnt(23)
s_barrier
ds_read_b128 a[208:211], v136 offset:24576
ds_read_b128 a[212:215], v136 offset:25600
v_mul_f32 v168, v140, v168
v_mfma_f32_32x32x16_bf16 a[128:143], v[92:95], a[228:231], a[128:143]
v_mul_f32 v169, v140, v169
v_mul_f32 v170, v140, v170
v_mul_f32 v171, v140, v171
v_cvt_pk_bf16_f32 v100, v168, v169
v_cvt_pk_bf16_f32 v101, v170, v171
v_mul_f32 v172, v140, v172
buffer_load_dwordx4 v8, s[28:31], s85 offen offset:1024 lds
v_mfma_f32_32x32x16_bf16 v[176:191], v[148:151], v[108:111], v[176:191]
v_mul_f32 v173, v140, v173
v_mul_f32 v174, v140, v174
v_mul_f32 v175, v140, v175
v_cvt_pk_bf16_f32 v102, v172, v173
v_cvt_pk_bf16_f32 v103, v174, v175
s_waitcnt lgkmcnt(0)
v_mfma_f32_32x32x16_bf16 a[112:127], v[96:99], a[208:211], a[112:127]
ds_read_b128 a[224:227], v137 offset:24576
ds_read_b128 a[228:231], v137 offset:25600
s_add_u32 m0, s84, 0x20000
s_nop 0
buffer_load_dwordx4 v7, s[32:35], s85 offen lds
v_mfma_f32_32x32x16_bf16 a[128:143], v[96:99], a[212:215], a[128:143]
v_mul_f32 v176, v140, v176
v_mul_f32 v177, v140, v177
v_mul_f32 v178, v140, v178
v_mul_f32 v179, v140, v179
v_cvt_pk_bf16_f32 v104, v176, v177
v_cvt_pk_bf16_f32 v105, v178, v179
v_mul_f32 v180, v140, v180
v_mfma_f32_32x32x16_bf16 v[160:175], v[144:147], v[112:115], v[192:207]
v_mul_f32 v181, v140, v181
v_mul_f32 v182, v140, v182
v_mul_f32 v183, v140, v183
v_cvt_pk_bf16_f32 v106, v180, v181
v_cvt_pk_bf16_f32 v107, v182, v183
buffer_load_dwordx4 v8, s[32:35], s85 offen offset:1024 lds
s_waitcnt lgkmcnt(0)
v_mfma_f32_32x32x16_bf16 a[112:127], v[100:103], a[224:227], a[112:127]
ds_read_b128 a[208:211], v138 offset:24576
ds_read_b128 a[212:215], v138 offset:25600
v_mul_f32 v184, v140, v184
v_mul_f32 v185, v140, v185
v_mul_f32 v186, v140, v186
v_mfma_f32_32x32x16_bf16 a[128:143], v[100:103], a[228:231], a[128:143]
ds_read_b128 a[24:27], v13 offset:24576
v_mul_f32 v187, v140, v187
v_cvt_pk_bf16_f32 v108, v184, v185
v_cvt_pk_bf16_f32 v109, v186, v187
v_mul_f32 v188, v140, v188
v_mul_f32 v189, v140, v189
s_add_u32 m0, s84, 0
s_mov_b32 s87, 0x80
buffer_load_dwordx4 v9, s[40:43], s87 offen lds
v_mfma_f32_32x32x16_bf16 v[160:175], v[148:151], v[116:119], v[160:175]
ds_read_b128 a[28:31], v13 offset:28672
v_mul_f32 v190, v140, v190
v_mul_f32 v191, v140, v191
v_cvt_pk_bf16_f32 v110, v188, v189
v_cvt_pk_bf16_f32 v111, v190, v191
buffer_load_dwordx4 v10, s[40:43], s87 offen offset:1024 lds
s_waitcnt lgkmcnt(2)
v_mfma_f32_32x32x16_bf16 a[112:127], v[104:107], a[208:211], a[112:127]
ds_read_b128 a[224:227], v139 offset:24576
ds_read_b128 a[228:231], v139 offset:25600
v_mfma_f32_32x32x16_bf16 a[128:143], v[104:107], a[212:215], a[128:143]
s_nop 0
v_mul_f32 v160, v140, v160
v_mul_f32 v161, v140, v161
v_mul_f32 v162, v140, v162
v_mul_f32 v163, v140, v163
v_cvt_pk_bf16_f32 v112, v160, v161
v_cvt_pk_bf16_f32 v113, v162, v163
v_mul_f32 v164, v140, v164
v_mfma_f32_32x32x16_bf16 v[176:191], v[144:147], v[120:123], v[192:207]
v_mul_f32 v165, v140, v165
v_mul_f32 v166, v140, v166
v_mul_f32 v167, v140, v167
v_cvt_pk_bf16_f32 v114, v164, v165
v_cvt_pk_bf16_f32 v115, v166, v167
s_add_u32 m0, s84, 0x22000
s_mul_i32 s85, s76, 0x38000
buffer_load_dwordx4 v7, s[28:31], s85 offen lds
s_waitcnt lgkmcnt(0)
v_mfma_f32_32x32x16_bf16 a[112:127], v[108:111], a[224:227], a[112:127]
s_waitcnt vmcnt(25)
s_barrier
ds_read_b128 a[208:211], v128 offset:32768
ds_read_b128 a[212:215], v128 offset:33792
v_mul_f32 v168, v140, v168
buffer_load_dwordx4 v8, s[28:31], s85 offen offset:1024 lds
v_mfma_f32_32x32x16_bf16 a[128:143], v[108:111], a[228:231], a[128:143]
v_mul_f32 v169, v140, v169
v_mul_f32 v170, v140, v170
v_mul_f32 v171, v140, v171
v_cvt_pk_bf16_f32 v116, v168, v169
v_cvt_pk_bf16_f32 v117, v170, v171
v_mul_f32 v172, v140, v172
v_mfma_f32_32x32x16_bf16 v[176:191], v[148:151], v[124:127], v[176:191]
v_mul_f32 v173, v140, v173
v_mul_f32 v174, v140, v174
v_mul_f32 v175, v140, v175
v_cvt_pk_bf16_f32 v118, v172, v173
v_cvt_pk_bf16_f32 v119, v174, v175
s_add_u32 m0, s84, 0x2000
s_nop 0
buffer_load_dwordx4 v7, s[32:35], s85 offen lds
s_waitcnt lgkmcnt(0)
v_mfma_f32_32x32x16_bf16 a[112:127], v[112:115], a[208:211], a[112:127]
ds_read_b128 a[224:227], v129 offset:32768
ds_read_b128 a[228:231], v129 offset:33792
buffer_load_dwordx4 v8, s[32:35], s85 offen offset:1024 lds
v_mfma_f32_32x32x16_bf16 a[128:143], v[112:115], a[212:215], a[128:143]
v_mul_f32 v176, v140, v176
v_mul_f32 v177, v140, v177
v_mul_f32 v178, v140, v178
v_mul_f32 v179, v140, v179
v_cvt_pk_bf16_f32 v120, v176, v177
v_cvt_pk_bf16_f32 v121, v178, v179
v_mul_f32 v180, v140, v180
v_mul_f32 v181, v140, v181
v_mul_f32 v182, v140, v182
v_mul_f32 v183, v140, v183
s_waitcnt lgkmcnt(0)
v_mfma_f32_32x32x16_bf16 a[112:127], v[116:119], a[224:227], a[112:127]
ds_read_b128 a[208:211], v130 offset:32768
ds_read_b128 a[212:215], v130 offset:33792
v_cvt_pk_bf16_f32 v122, v180, v181
v_cvt_pk_bf16_f32 v123, v182, v183
v_mul_f32 v184, v140, v184
v_mul_f32 v185, v140, v185
v_mul_f32 v186, v140, v186
v_mul_f32 v187, v140, v187
v_cvt_pk_bf16_f32 v124, v184, v185
s_add_u32 m0, s84, 0x24000
s_mov_b32 s87, 0x100
buffer_load_dwordx4 v9, s[40:43], s87 offen lds
v_mfma_f32_32x32x16_bf16 a[128:143], v[116:119], a[228:231], a[128:143]
ds_read_b128 a[32:35], v13 offset:40960
ds_read_b128 a[36:39], v13 offset:45056
v_cvt_pk_bf16_f32 v125, v186, v187
v_mul_f32 v188, v140, v188
v_mul_f32 v189, v140, v189
v_mul_f32 v190, v140, v190
v_mul_f32 v191, v140, v191
v_cvt_pk_bf16_f32 v126, v188, v189
v_cvt_pk_bf16_f32 v127, v190, v191
s_waitcnt lgkmcnt(2)
v_mfma_f32_32x32x16_bf16 a[112:127], v[120:123], a[208:211], a[112:127]
ds_read_b128 a[224:227], v131 offset:32768
ds_read_b128 a[228:231], v131 offset:33792
buffer_load_dwordx4 v10, s[40:43], s87 offen offset:1024 lds
v_mfma_f32_32x32x16_bf16 a[128:143], v[120:123], a[212:215], a[128:143]
s_waitcnt lgkmcnt(0)
v_mfma_f32_32x32x16_bf16 a[112:127], v[124:127], a[224:227], a[112:127]
s_waitcnt vmcnt(24)
s_barrier
ds_read_b64_tr_b16 a[208:209], v1 offset:49152
ds_read_b64_tr_b16 a[210:211], v1 offset:49664
ds_read_b64_tr_b16 a[212:213], v4 offset:49152
ds_read_b64_tr_b16 a[214:215], v4 offset:49664
s_add_u32 m0, s84, 0x4000
s_mul_i32 s85, s77, 0x38000
buffer_load_dwordx4 v7, s[28:31], s85 offen lds
v_mfma_f32_32x32x16_bf16 a[128:143], v[124:127], a[228:231], a[128:143]
ds_read_b64_tr_b16 a[216:217], v1 offset:57344
ds_read_b64_tr_b16 a[218:219], v1 offset:57856
ds_read_b64_tr_b16 a[220:221], v4 offset:57344
ds_read_b64_tr_b16 a[222:223], v4 offset:57856
buffer_load_dwordx4 v8, s[28:31], s85 offen offset:1024 lds
s_waitcnt lgkmcnt(4)
v_mfma_f32_32x32x16_bf16 a[144:159], a[208:211], v[16:19], 0
ds_read_b64_tr_b16 a[224:225], v1 offset:51200
ds_read_b64_tr_b16 a[226:227], v1 offset:51712
ds_read_b64_tr_b16 a[228:229], v4 offset:51200
v_mfma_f32_32x32x16_bf16 a[160:175], a[212:215], v[16:19], 0
ds_read_b64_tr_b16 a[230:231], v4 offset:51712
ds_read_b64_tr_b16 a[232:233], v1 offset:59392
ds_read_b64_tr_b16 a[234:235], v1 offset:59904
s_add_u32 m0, s84, 0x26000
s_nop 0
buffer_load_dwordx4 v7, s[32:35], s85 offen lds
s_waitcnt lgkmcnt(6)
v_mfma_f32_32x32x16_bf16 a[176:191], a[216:219], v[16:19], 0
ds_read_b64_tr_b16 a[236:237], v4 offset:59392
ds_read_b64_tr_b16 a[238:239], v4 offset:59904
v_accvgpr_read_b32 v160, a112
v_accvgpr_read_b32 v161, a113
v_accvgpr_read_b32 v162, a114
v_accvgpr_read_b32 v163, a115
buffer_load_dwordx4 v8, s[32:35], s85 offen offset:1024 lds
v_mfma_f32_32x32x16_bf16 a[192:207], a[220:223], v[16:19], 0
v_accvgpr_read_b32 v176, a128
v_accvgpr_read_b32 v177, a129
v_accvgpr_read_b32 v178, a130
v_accvgpr_read_b32 v179, a131
v_fma_f32 v212, v224, v160, v161
s_waitcnt lgkmcnt(4)
v_mfma_f32_32x32x16_bf16 a[144:159], a[224:227], v[20:23], a[144:159]
ds_read_b64_tr_b16 a[208:209], v1 offset:53248
ds_read_b64_tr_b16 a[210:211], v1 offset:53760
ds_read_b64_tr_b16 a[212:213], v4 offset:53248
v_mov_b32 v250, 0
v_fma_f32 v213, v224, v176, v177
v_fma_f32 v212, -v225, v176, v212
v_mfma_f32_32x32x16_bf16 a[160:175], a[228:231], v[20:23], a[160:175]
ds_read_b64_tr_b16 a[214:215], v4 offset:53760
ds_read_b64_tr_b16 a[216:217], v1 offset:61440
ds_read_b64_tr_b16 a[218:219], v1 offset:61952
v_fma_f32 v213, v225, v160, v213
v_fma_f32 v214, v224, v212, v162
s_waitcnt lgkmcnt(6)
v_mfma_f32_32x32x16_bf16 a[176:191], a[232:235], v[20:23], a[176:191]
ds_read_b64_tr_b16 a[220:221], v4 offset:61440
ds_read_b64_tr_b16 a[222:223], v4 offset:61952
v_mov_b32 v251, 0
ds_write_b64 v239, v[250:251] offset:0
v_fma_f32 v215, v224, v213, v178
v_fma_f32 v214, -v225, v213, v214
s_add_u32 m0, s84, 0x6000
s_mov_b32 s87, 0x180
buffer_load_dwordx4 v9, s[40:43], s87 offen lds
v_mfma_f32_32x32x16_bf16 a[192:207], a[236:239], v[20:23], a[192:207]
ds_read_b128 a[40:43], v14 offset:0
ds_read_b128 a[44:47], v14 offset:4096
v_fma_f32 v215, v225, v212, v215
v_fma_f32 v192, v224, v214, v163
v_fma_f32 v193, v224, v215, v179
buffer_load_dwordx4 v10, s[40:43], s87 offen offset:1024 lds
s_waitcnt lgkmcnt(7)
v_mfma_f32_32x32x16_bf16 a[144:159], a[208:211], v[24:27], a[144:159]
ds_read_b64_tr_b16 a[224:225], v1 offset:55296
ds_read_b64_tr_b16 a[226:227], v1 offset:55808
ds_read_b64_tr_b16 a[228:229], v4 offset:55296
ds_read_b64_tr_b16 a[230:231], v4 offset:55808
v_fma_f32 v192, -v225, v215, v192
v_fma_f32 v193, v225, v214, v193
v_mfma_f32_32x32x16_bf16 a[160:175], a[212:215], v[24:27], a[160:175]
ds_read_b64_tr_b16 a[232:233], v1 offset:63488
ds_read_b64_tr_b16 a[234:235], v1 offset:64000
ds_read_b64_tr_b16 a[236:237], v4 offset:63488
ds_read_b64_tr_b16 a[238:239], v4 offset:64000
v_mov_b32 v194, v192
s_add_u32 m0, s84, 0x8000
s_mul_i32 s85, s78, 0x38000
buffer_load_dwordx4 v7, s[28:31], s85 offen lds
s_waitcnt lgkmcnt(11)
v_mfma_f32_32x32x16_bf16 a[176:191], a[216:219], v[24:27], a[176:191]
v_mov_b32 v195, v193
v_accvgpr_read_b32 v164, a116
v_accvgpr_read_b32 v165, a117
v_accvgpr_read_b32 v166, a118
v_accvgpr_read_b32 v167, a119
v_accvgpr_read_b32 v180, a132
buffer_load_dwordx4 v8, s[28:31], s85 offen offset:1024 lds
v_mfma_f32_32x32x16_bf16 a[192:207], a[220:223], v[24:27], a[192:207]
v_accvgpr_read_b32 v181, a133
v_accvgpr_read_b32 v182, a134
v_accvgpr_read_b32 v183, a135
v_fma_f32 v212, v224, v164, v165
s_waitcnt lgkmcnt(0)
v_mfma_f32_32x32x16_bf16 a[144:159], a[224:227], v[28:31], a[144:159]
s_waitcnt vmcnt(26)
s_barrier
ds_read_b64_tr_b16 a[208:209], v2 offset:8192
ds_read_b64_tr_b16 a[210:211], v2 offset:8704
ds_read_b64_tr_b16 a[212:213], v5 offset:8192
v_mfma_f32_32x32x16_bf16 a[160:175], a[228:231], v[28:31], a[160:175]
ds_read_b64_tr_b16 a[214:215], v5 offset:8704
ds_read_b64_tr_b16 a[216:217], v2 offset:16384
ds_read_b64_tr_b16 a[218:219], v2 offset:16896
v_fma_f32 v213, v224, v180, v181
v_fma_f32 v212, -v225, v180, v212
s_add_u32 m0, s84, 0x10000
s_nop 0
buffer_load_dwordx4 v7, s[32:35], s85 offen lds
v_mfma_f32_32x32x16_bf16 a[176:191], a[232:235], v[28:31], a[176:191]
ds_read_b64_tr_b16 a[220:221], v5 offset:16384
ds_read_b64_tr_b16 a[222:223], v5 offset:16896
v_fma_f32 v213, v225, v164, v213
v_fma_f32 v214, v224, v212, v166
v_fma_f32 v215, v224, v213, v182
v_fma_f32 v214, -v225, v213, v214
buffer_load_dwordx4 v8, s[32:35], s85 offen offset:1024 lds
v_mfma_f32_32x32x16_bf16 a[192:207], a[236:239], v[28:31], a[192:207]
v_fma_f32 v215, v225, v212, v215
v_fma_f32 v196, v224, v214, v167
v_fma_f32 v197, v224, v215, v183
v_fma_f32 v196, -v225, v215, v196
v_fma_f32 v197, v225, v214, v197
s_waitcnt lgkmcnt(4)
v_mfma_f32_32x32x16_bf16 a[144:159], a[208:211], v[32:35], a[144:159]
ds_read_b64_tr_b16 a[224:225], v2 offset:10240
ds_read_b64_tr_b16 a[226:227], v2 offset:10752
ds_read_b64_tr_b16 a[228:229], v5 offset:10240
v_mov_b32 v198, v196
v_mov_b32 v199, v197
v_accvgpr_read_b32 v168, a120
v_mfma_f32_32x32x16_bf16 a[160:175], a[212:215], v[32:35], a[160:175]
ds_read_b64_tr_b16 a[230:231], v5 offset:10752
ds_read_b64_tr_b16 a[232:233], v2 offset:18432
ds_read_b64_tr_b16 a[234:235], v2 offset:18944
v_accvgpr_read_b32 v169, a121
v_accvgpr_read_b32 v170, a122
s_add_u32 m0, s84, 0xe000
s_mov_b32 s87, 0x200
buffer_load_dwordx4 v9, s[40:43], s87 offen lds
s_waitcnt lgkmcnt(6)
v_mfma_f32_32x32x16_bf16 a[176:191], a[216:219], v[32:35], a[176:191]
ds_read_b64_tr_b16 a[236:237], v5 offset:18432
ds_read_b64_tr_b16 a[238:239], v5 offset:18944
v_accvgpr_read_b32 v171, a123
v_accvgpr_read_b32 v184, a136
v_accvgpr_read_b32 v185, a137
v_accvgpr_read_b32 v186, a138
buffer_load_dwordx4 v10, s[40:43], s87 offen offset:1024 lds
v_mfma_f32_32x32x16_bf16 a[192:207], a[220:223], v[32:35], a[192:207]
v_accvgpr_read_b32 v187, a139
v_fma_f32 v212, v224, v168, v169
v_fma_f32 v213, v224, v184, v185
v_fma_f32 v212, -v225, v184, v212
v_fma_f32 v213, v225, v168, v213
s_waitcnt lgkmcnt(4)
v_mfma_f32_32x32x16_bf16 a[144:159], a[224:227], v[36:39], a[144:159]
ds_read_b64_tr_b16 a[208:209], v2 offset:12288
ds_read_b64_tr_b16 a[210:211], v2 offset:12800
ds_read_b64_tr_b16 a[212:213], v5 offset:12288
v_fma_f32 v214, v224, v212, v170
v_fma_f32 v215, v224, v213, v186
v_fma_f32 v214, -v225, v213, v214
v_add_u32 v7, 0x80, v7
v_add_u32 v8, 0x80, v8
s_add_u32 m0, s84, 0xc000
s_mul_i32 s85, s72, 0x38000
buffer_load_dwordx4 v7, s[28:31], s85 offen lds
v_mfma_f32_32x32x16_bf16 a[160:175], a[228:231], v[36:39], a[160:175]
ds_read_b64_tr_b16 a[214:215], v5 offset:12800
ds_read_b64_tr_b16 a[216:217], v2 offset:20480
ds_read_b64_tr_b16 a[218:219], v2 offset:20992
v_fma_f32 v215, v225, v212, v215
v_fma_f32 v200, v224, v214, v171
s_waitcnt lgkmcnt(6)
v_mfma_f32_32x32x16_bf16 a[176:191], a[232:235], v[36:39], a[176:191]
ds_read_b64_tr_b16 a[220:221], v5 offset:20480
ds_read_b64_tr_b16 a[222:223], v5 offset:20992
v_fma_f32 v201, v224, v215, v187
v_fma_f32 v200, -v225, v215, v200
v_fma_f32 v201, v225, v214, v201
buffer_load_dwordx4 v8, s[28:31], s85 offen offset:1024 lds
v_mfma_f32_32x32x16_bf16 a[192:207], a[236:239], v[36:39], a[192:207]
ds_read_b128 a[48:51], v14 offset:24576
ds_read_b128 a[52:55], v14 offset:28672
v_mov_b32 v202, v200
v_mov_b32 v203, v201
v_accvgpr_read_b32 v172, a124
s_waitcnt lgkmcnt(6)
v_mfma_f32_32x32x16_bf16 a[144:159], a[208:211], v[40:43], a[144:159]
ds_read_b64_tr_b16 a[224:225], v2 offset:14336
ds_read_b64_tr_b16 a[226:227], v2 offset:14848
ds_read_b64_tr_b16 a[228:229], v5 offset:14336
ds_read_b64_tr_b16 a[230:231], v5 offset:14848
v_accvgpr_read_b32 v173, a125
v_accvgpr_read_b32 v174, a126
v_mfma_f32_32x32x16_bf16 a[160:175], a[212:215], v[40:43], a[160:175]
ds_read_b64_tr_b16 a[232:233], v2 offset:22528
ds_read_b64_tr_b16 a[234:235], v2 offset:23040
ds_read_b64_tr_b16 a[236:237], v5 offset:22528
ds_read_b64_tr_b16 a[238:239], v5 offset:23040
v_accvgpr_read_b32 v175, a127
s_waitcnt lgkmcnt(10)
v_mfma_f32_32x32x16_bf16 a[176:191], a[216:219], v[40:43], a[176:191]
v_accvgpr_read_b32 v188, a140
v_accvgpr_read_b32 v189, a141
v_accvgpr_read_b32 v190, a142
v_accvgpr_read_b32 v191, a143
v_fma_f32 v212, v224, v172, v173
v_fma_f32 v213, v224, v188, v189
v_mfma_f32_32x32x16_bf16 a[192:207], a[220:223], v[40:43], a[192:207]
v_fma_f32 v212, -v225, v188, v212
v_fma_f32 v213, v225, v172, v213
v_fma_f32 v214, v224, v212, v174
v_fma_f32 v215, v224, v213, v190
v_fma_f32 v214, -v225, v213, v214
s_waitcnt lgkmcnt(0)
v_mfma_f32_32x32x16_bf16 a[144:159], a[224:227], v[44:47], a[144:159]
s_waitcnt vmcnt(26)
s_barrier
ds_read_b64_tr_b16 a[208:209], v2 offset:32768
ds_read_b64_tr_b16 a[210:211], v2 offset:33280
ds_read_b64_tr_b16 a[212:213], v5 offset:32768
s_add_u32 m0, s84, 0x16000
s_nop 0
buffer_load_dwordx4 v7, s[32:35], s85 offen lds
v_mfma_f32_32x32x16_bf16 a[160:175], a[228:231], v[44:47], a[160:175]
ds_read_b64_tr_b16 a[214:215], v5 offset:33280
ds_read_b64_tr_b16 a[216:217], v2 offset:40960
ds_read_b64_tr_b16 a[218:219], v2 offset:41472
v_fma_f32 v215, v225, v212, v215
v_fma_f32 v204, v224, v214, v175
buffer_load_dwordx4 v8, s[32:35], s85 offen offset:1024 lds
v_mfma_f32_32x32x16_bf16 a[176:191], a[232:235], v[44:47], a[176:191]
ds_read_b64_tr_b16 a[220:221], v5 offset:40960
ds_read_b64_tr_b16 a[222:223], v5 offset:41472
v_fma_f32 v205, v224, v215, v191
v_fma_f32 v204, -v225, v215, v204
v_fma_f32 v205, v225, v214, v205
v_mov_b32 v206, v204
v_mfma_f32_32x32x16_bf16 a[192:207], a[236:239], v[44:47], a[192:207]
v_mov_b32 v207, v205
v_permlane32_swap_b32 v192, v194
v_permlane32_swap_b32 v193, v195
v_permlane32_swap_b32 v196, v198
v_permlane32_swap_b32 v197, v199
s_add_u32 m0, s84, 0x14000
s_mov_b32 s87, 0x280
buffer_load_dwordx4 v9, s[40:43], s87 offen lds
s_waitcnt lgkmcnt(4)
v_mfma_f32_32x32x16_bf16 a[144:159], a[208:211], v[48:51], a[144:159]
ds_read_b64_tr_b16 a[224:225], v2 offset:34816
ds_read_b64_tr_b16 a[226:227], v2 offset:35328
ds_read_b64_tr_b16 a[228:229], v5 offset:34816
v_permlane32_swap_b32 v200, v202
v_permlane32_swap_b32 v201, v203
v_permlane32_swap_b32 v204, v206
buffer_load_dwordx4 v10, s[40:43], s87 offen offset:1024 lds
v_mfma_f32_32x32x16_bf16 a[160:175], a[212:215], v[48:51], a[160:175]
ds_read_b64_tr_b16 a[230:231], v5 offset:35328
ds_read_b64_tr_b16 a[232:233], v2 offset:43008
ds_read_b64_tr_b16 a[234:235], v2 offset:43520
v_permlane32_swap_b32 v205, v207
s_waitcnt lgkmcnt(6)
v_mfma_f32_32x32x16_bf16 a[176:191], a[216:219], v[48:51], a[176:191]
ds_read_b64_tr_b16 a[236:237], v5 offset:43008
ds_read_b64_tr_b16 a[238:239], v5 offset:43520
v_fma_f32 v212, v226, v192, v194
v_fma_f32 v213, v226, v193, v195
v_fma_f32 v212, -v227, v193, v212
v_fma_f32 v213, v227, v192, v213
v_mfma_f32_32x32x16_bf16 a[192:207], a[220:223], v[48:51], a[192:207]
v_fma_f32 v214, v226, v212, v196
v_fma_f32 v215, v226, v213, v197
v_fma_f32 v214, -v227, v213, v214
v_fma_f32 v215, v227, v212, v215
v_fma_f32 v212, v226, v214, v198
s_add_u32 m0, s84, 0x12000
s_mov_b32 s87, 0
buffer_load_dwordx4 v11, s[44:47], s87 offen lds
s_waitcnt lgkmcnt(4)
v_mfma_f32_32x32x16_bf16 a[144:159], a[224:227], v[52:55], a[144:159]
ds_read_b64_tr_b16 a[208:209], v2 offset:36864
ds_read_b64_tr_b16 a[210:211], v2 offset:37376
ds_read_b64_tr_b16 a[212:213], v5 offset:36864
v_fma_f32 v213, v226, v215, v199
v_fma_f32 v212, -v227, v215, v212
v_fma_f32 v213, v227, v214, v213
buffer_load_dwordx4 v12, s[44:47], s87 offen offset:1024 lds
v_mfma_f32_32x32x16_bf16 a[160:175], a[228:231], v[52:55], a[160:175]
ds_read_b64_tr_b16 a[214:215], v5 offset:37376
ds_read_b64_tr_b16 a[216:217], v2 offset:45056
ds_read_b64_tr_b16 a[218:219], v2 offset:45568
v_fma_f32 v214, v226, v212, v200
v_fma_f32 v215, v226, v213, v201
s_waitcnt lgkmcnt(6)
v_mfma_f32_32x32x16_bf16 a[176:191], a[232:235], v[52:55], a[176:191]
ds_read_b64_tr_b16 a[220:221], v5 offset:45056
ds_read_b64_tr_b16 a[222:223], v5 offset:45568
v_fma_f32 v214, -v227, v213, v214
v_fma_f32 v215, v227, v212, v215
v_fma_f32 v212, v226, v214, v202
v_fma_f32 v213, v226, v215, v203
v_mfma_f32_32x32x16_bf16 a[192:207], a[236:239], v[52:55], a[192:207]
ds_read_b128 a[56:59], v14 offset:49152
ds_read_b128 a[60:63], v14 offset:53248
v_fma_f32 v212, -v227, v215, v212
v_fma_f32 v213, v227, v214, v213
v_fma_f32 v214, v226, v212, v204
s_waitcnt lgkmcnt(6)
v_mfma_f32_32x32x16_bf16 a[144:159], a[208:211], v[56:59], a[144:159]
ds_read_b64_tr_b16 a[224:225], v2 offset:38912
ds_read_b64_tr_b16 a[226:227], v2 offset:39424
ds_read_b64_tr_b16 a[228:229], v5 offset:38912
ds_read_b64_tr_b16 a[230:231], v5 offset:39424
v_fma_f32 v215, v226, v213, v205
v_fma_f32 v214, -v227, v213, v214
v_mfma_f32_32x32x16_bf16 a[160:175], a[212:215], v[56:59], a[160:175]
ds_read_b64_tr_b16 a[232:233], v2 offset:47104
ds_read_b64_tr_b16 a[234:235], v2 offset:47616
ds_read_b64_tr_b16 a[236:237], v5 offset:47104
ds_read_b64_tr_b16 a[238:239], v5 offset:47616
v_fma_f32 v215, v227, v212, v215
s_waitcnt lgkmcnt(10)
v_mfma_f32_32x32x16_bf16 a[176:191], a[216:219], v[56:59], a[176:191]
v_fma_f32 v208, v226, v214, v206
v_fma_f32 v209, v226, v215, v207
v_fma_f32 v208, -v227, v215, v208
v_fma_f32 v209, v227, v214, v209
ds_write_b64 v239, v[208:209] offset:768
v_mfma_f32_32x32x16_bf16 a[192:207], a[220:223], v[56:59], a[192:207]
s_waitcnt lgkmcnt(1)
v_mfma_f32_32x32x16_bf16 a[144:159], a[224:227], v[60:63], a[144:159]
s_waitcnt lgkmcnt(0)
s_waitcnt vmcnt(26)
s_barrier
ds_read_b64_tr_b16 a[208:209], v2 offset:57344
ds_read_b64_tr_b16 a[210:211], v2 offset:57856
ds_read_b64_tr_b16 a[212:213], v5 offset:57344
s_add_u32 m0, s84, 0x1c000
s_mul_i32 s85, s73, 0x38000
buffer_load_dwordx4 v7, s[28:31], s85 offen lds
v_mfma_f32_32x32x16_bf16 a[160:175], a[228:231], v[60:63], a[160:175]
ds_read_b64_tr_b16 a[214:215], v5 offset:57856
ds_read_b64_tr_b16 a[216:217], v3 offset:0
ds_read_b64_tr_b16 a[218:219], v3 offset:512
ds_read_b64 v[216:217], v239 offset:0
buffer_load_dwordx4 v8, s[28:31], s85 offen offset:1024 lds
v_mfma_f32_32x32x16_bf16 a[176:191], a[232:235], v[60:63], a[176:191]
ds_read_b64_tr_b16 a[220:221], v6 offset:0
ds_read_b64_tr_b16 a[222:223], v6 offset:512
ds_read_b64 v[218:219], v239 offset:256
ds_read_b64 v[220:221], v239 offset:512
v_mfma_f32_32x32x16_bf16 a[192:207], a[236:239], v[60:63], a[192:207]
s_add_u32 m0, s84, 0x1a000
s_nop 0
buffer_load_dwordx4 v7, s[32:35], s85 offen lds
s_waitcnt lgkmcnt(7)
v_mfma_f32_32x32x16_bf16 a[144:159], a[208:211], v[64:67], a[144:159]
ds_read_b64_tr_b16 a[224:225], v2 offset:59392
ds_read_b64_tr_b16 a[226:227], v2 offset:59904
ds_read_b64_tr_b16 a[228:229], v5 offset:59392
s_waitcnt lgkmcnt(4)
v_fma_f32 v212, v228, v216, v218
v_fma_f32 v213, v228, v217, v219
v_fma_f32 v212, -v229, v217, v212
v_fma_f32 v213, v229, v216, v213
buffer_load_dwordx4 v8, s[32:35], s85 offen offset:1024 lds
v_mfma_f32_32x32x16_bf16 a[160:175], a[212:215], v[64:67], a[160:175]
ds_read_b64_tr_b16 a[230:231], v5 offset:59904
ds_read_b64_tr_b16 a[232:233], v3 offset:2048
ds_read_b64_tr_b16 a[234:235], v3 offset:2560
s_waitcnt lgkmcnt(6)
v_fma_f32 v210, v228, v212, v220
v_fma_f32 v211, v228, v213, v221
v_mfma_f32_32x32x16_bf16 a[176:191], a[216:219], v[64:67], a[176:191]
ds_read_b64_tr_b16 a[236:237], v6 offset:2048
ds_read_b64_tr_b16 a[238:239], v6 offset:2560
v_fma_f32 v210, -v229, v213, v210
v_fma_f32 v211, v229, v212, v211
v_fma_f32 v212, v228, v210, v208
v_fma_f32 v213, v228, v211, v209
v_mfma_f32_32x32x16_bf16 a[192:207], a[220:223], v[64:67], a[192:207]
v_fma_f32 v212, -v229, v211, v212
v_fma_f32 v213, v229, v210, v213
v_cndmask_b32 v214, v212, v213, s[12:13]
v_mov_b32 v215, s9
buffer_store_dwordx2 v[214:215], v240, s[80:83], 0 offen sc1
s_waitcnt lgkmcnt(4)
v_mfma_f32_32x32x16_bf16 a[144:159], a[224:227], v[68:71], a[144:159]
ds_read_b64_tr_b16 a[208:209], v2 offset:61440
ds_read_b64_tr_b16 a[210:211], v2 offset:61952
ds_read_b64_tr_b16 a[212:213], v5 offset:61440
v_mfma_f32_32x32x16_bf16 a[160:175], a[228:231], v[68:71], a[160:175]
ds_read_b64_tr_b16 a[214:215], v5 offset:61952
ds_read_b64_tr_b16 a[216:217], v3 offset:4096
ds_read_b64_tr_b16 a[218:219], v3 offset:4608
s_waitcnt lgkmcnt(6)
v_mfma_f32_32x32x16_bf16 a[176:191], a[232:235], v[68:71], a[176:191]
ds_read_b64_tr_b16 a[220:221], v6 offset:4096
ds_read_b64_tr_b16 a[222:223], v6 offset:4608
s_add_u32 m0, s84, 0x18000
s_mov_b32 s87, 0x300
buffer_load_dwordx4 v9, s[40:43], s87 offen lds
v_mfma_f32_32x32x16_bf16 a[192:207], a[236:239], v[68:71], a[192:207]
ds_read_b128 a[64:67], v13 offset:0
ds_read_b128 a[68:71], v13 offset:4096
s_waitcnt lgkmcnt(6)
v_mfma_f32_32x32x16_bf16 a[144:159], a[208:211], v[72:75], a[144:159]
ds_read_b64_tr_b16 a[224:225], v2 offset:63488
ds_read_b64_tr_b16 a[226:227], v2 offset:64000
ds_read_b64_tr_b16 a[228:229], v5 offset:63488
ds_read_b64_tr_b16 a[230:231], v5 offset:64000
buffer_load_dwordx4 v10, s[40:43], s87 offen offset:1024 lds
v_mfma_f32_32x32x16_bf16 a[160:175], a[212:215], v[72:75], a[160:175]
ds_read_b64_tr_b16 a[232:233], v3 offset:6144
ds_read_b64_tr_b16 a[234:235], v3 offset:6656
ds_read_b64_tr_b16 a[236:237], v6 offset:6144
ds_read_b64_tr_b16 a[238:239], v6 offset:6656
s_waitcnt lgkmcnt(10)
v_mfma_f32_32x32x16_bf16 a[176:191], a[216:219], v[72:75], a[176:191]
v_mfma_f32_32x32x16_bf16 a[192:207], a[220:223], v[72:75], a[192:207]
s_waitcnt lgkmcnt(0)
v_mfma_f32_32x32x16_bf16 a[144:159], a[224:227], v[76:79], a[144:159]
s_waitcnt vmcnt(27)
s_barrier
ds_read_b64_tr_b16 a[208:209], v3 offset:8192
ds_read_b64_tr_b16 a[210:211], v3 offset:8704
ds_read_b64_tr_b16 a[212:213], v6 offset:8192
s_add_u32 m0, s84, 0x20000
s_mov_b32 s87, 0x1c000
buffer_load_dwordx4 v11, s[44:47], s87 offen lds
v_mfma_f32_32x32x16_bf16 a[160:175], a[228:231], v[76:79], a[160:175]
ds_read_b64_tr_b16 a[214:215], v6 offset:8704
ds_read_b64_tr_b16 a[216:217], v1 offset:8192
ds_read_b64_tr_b16 a[218:219], v1 offset:8704
buffer_load_dwordx4 v12, s[44:47], s87 offen offset:1024 lds
v_mfma_f32_32x32x16_bf16 a[176:191], a[232:235], v[76:79], a[176:191]
ds_read_b64_tr_b16 a[220:221], v4 offset:8192
ds_read_b64_tr_b16 a[222:223], v4 offset:8704
v_mfma_f32_32x32x16_bf16 a[192:207], a[236:239], v[76:79], a[192:207]
s_add_u32 m0, s84, 0x1e000
s_mov_b32 s87, 0x38000
buffer_load_dwordx4 v11, s[44:47], s87 offen lds
s_waitcnt lgkmcnt(4)
v_mfma_f32_32x32x16_bf16 a[144:159], a[208:211], v[80:83], a[144:159]
ds_read_b64_tr_b16 a[224:225], v3 offset:10240
ds_read_b64_tr_b16 a[226:227], v3 offset:10752
ds_read_b64_tr_b16 a[228:229], v6 offset:10240
buffer_load_dwordx4 v12, s[44:47], s87 offen offset:1024 lds
v_mfma_f32_32x32x16_bf16 a[160:175], a[212:215], v[80:83], a[160:175]
ds_read_b64_tr_b16 a[230:231], v6 offset:10752
ds_read_b64_tr_b16 a[232:233], v1 offset:10240
ds_read_b64_tr_b16 a[234:235], v1 offset:10752
s_waitcnt lgkmcnt(6)
v_mfma_f32_32x32x16_bf16 a[176:191], a[216:219], v[80:83], a[176:191]
ds_read_b64_tr_b16 a[236:237], v4 offset:10240
ds_read_b64_tr_b16 a[238:239], v4 offset:10752
v_mfma_f32_32x32x16_bf16 a[192:207], a[220:223], v[80:83], a[192:207]
s_waitcnt lgkmcnt(4)
v_mfma_f32_32x32x16_bf16 a[144:159], a[224:227], v[84:87], a[144:159]
ds_read_b64_tr_b16 a[208:209], v3 offset:12288
ds_read_b64_tr_b16 a[210:211], v3 offset:12800
ds_read_b64_tr_b16 a[212:213], v6 offset:12288
s_add_u32 m0, s84, 0
s_mov_b32 s87, 0x54000
buffer_load_dwordx4 v11, s[44:47], s87 offen lds
v_mfma_f32_32x32x16_bf16 a[160:175], a[228:231], v[84:87], a[160:175]
ds_read_b64_tr_b16 a[214:215], v6 offset:12800
ds_read_b64_tr_b16 a[216:217], v1 offset:12288
ds_read_b64_tr_b16 a[218:219], v1 offset:12800
s_waitcnt lgkmcnt(6)
v_mfma_f32_32x32x16_bf16 a[176:191], a[232:235], v[84:87], a[176:191]
ds_read_b64_tr_b16 a[220:221], v4 offset:12288
ds_read_b64_tr_b16 a[222:223], v4 offset:12800
buffer_load_dwordx4 v12, s[44:47], s87 offen offset:1024 lds
v_mfma_f32_32x32x16_bf16 a[192:207], a[236:239], v[84:87], a[192:207]
ds_read_b128 a[72:75], v15 offset:16384
ds_read_b128 a[76:79], v15 offset:20480
s_waitcnt lgkmcnt(6)
v_mfma_f32_32x32x16_bf16 a[144:159], a[208:211], v[88:91], a[144:159]
ds_read_b64_tr_b16 a[224:225], v3 offset:14336
ds_read_b64_tr_b16 a[226:227], v3 offset:14848
ds_read_b64_tr_b16 a[228:229], v6 offset:14336
ds_read_b64_tr_b16 a[230:231], v6 offset:14848
v_mfma_f32_32x32x16_bf16 a[160:175], a[212:215], v[88:91], a[160:175]
ds_read_b64_tr_b16 a[232:233], v1 offset:14336
ds_read_b64_tr_b16 a[234:235], v1 offset:14848
ds_read_b64_tr_b16 a[236:237], v4 offset:14336
ds_read_b64_tr_b16 a[238:239], v4 offset:14848
s_waitcnt lgkmcnt(10)
v_mfma_f32_32x32x16_bf16 a[176:191], a[216:219], v[88:91], a[176:191]
v_mfma_f32_32x32x16_bf16 a[192:207], a[220:223], v[88:91], a[192:207]
s_waitcnt lgkmcnt(0)
v_mfma_f32_32x32x16_bf16 a[144:159], a[224:227], v[92:95], a[144:159]
s_waitcnt vmcnt(27)
s_barrier
ds_read_b64_tr_b16 a[208:209], v1 offset:16384
ds_read_b64_tr_b16 a[210:211], v1 offset:16896
ds_read_b64_tr_b16 a[212:213], v4 offset:16384
s_add_u32 m0, s84, 0x24000
s_mul_i32 s85, s74, 0x38000
buffer_load_dwordx4 v7, s[28:31], s85 offen lds
v_mfma_f32_32x32x16_bf16 a[160:175], a[228:231], v[92:95], a[160:175]
ds_read_b64_tr_b16 a[214:215], v4 offset:16896
ds_read_b64_tr_b16 a[216:217], v3 offset:24576
ds_read_b64_tr_b16 a[218:219], v3 offset:25088
buffer_load_dwordx4 v8, s[28:31], s85 offen offset:1024 lds
v_mfma_f32_32x32x16_bf16 a[176:191], a[232:235], v[92:95], a[176:191]
ds_read_b64_tr_b16 a[220:221], v6 offset:24576
ds_read_b64_tr_b16 a[222:223], v6 offset:25088
v_mfma_f32_32x32x16_bf16 a[192:207], a[236:239], v[92:95], a[192:207]
s_add_u32 m0, s84, 0x22000
s_nop 0
buffer_load_dwordx4 v7, s[32:35], s85 offen lds
s_waitcnt lgkmcnt(4)
v_mfma_f32_32x32x16_bf16 a[144:159], a[208:211], v[96:99], a[144:159]
ds_read_b64_tr_b16 a[224:225], v1 offset:18432
ds_read_b64_tr_b16 a[226:227], v1 offset:18944
ds_read_b64_tr_b16 a[228:229], v4 offset:18432
buffer_load_dwordx4 v8, s[32:35], s85 offen offset:1024 lds
v_mfma_f32_32x32x16_bf16 a[160:175], a[212:215], v[96:99], a[160:175]
ds_read_b64_tr_b16 a[230:231], v4 offset:18944
ds_read_b64_tr_b16 a[232:233], v3 offset:26624
ds_read_b64_tr_b16 a[234:235], v3 offset:27136
buffer_load_dwordx4 v[128:131], v241, s[80:83], 0 offen sc1
s_waitcnt lgkmcnt(6)
v_mfma_f32_32x32x16_bf16 a[176:191], a[216:219], v[96:99], a[176:191]
ds_read_b64_tr_b16 a[236:237], v6 offset:26624
ds_read_b64_tr_b16 a[238:239], v6 offset:27136
v_mfma_f32_32x32x16_bf16 a[192:207], a[220:223], v[96:99], a[192:207]
buffer_load_dwordx4 v[132:135], v241, s[80:83], 0 offen offset:1024 sc1
s_waitcnt lgkmcnt(4)
v_mfma_f32_32x32x16_bf16 a[144:159], a[224:227], v[100:103], a[144:159]
ds_read_b64_tr_b16 a[208:209], v1 offset:20480
ds_read_b64_tr_b16 a[210:211], v1 offset:20992
ds_read_b64_tr_b16 a[212:213], v4 offset:20480
v_mfma_f32_32x32x16_bf16 a[160:175], a[228:231], v[100:103], a[160:175]
ds_read_b64_tr_b16 a[214:215], v4 offset:20992
ds_read_b64_tr_b16 a[216:217], v3 offset:28672
ds_read_b64_tr_b16 a[218:219], v3 offset:29184
s_waitcnt lgkmcnt(6)
v_mfma_f32_32x32x16_bf16 a[176:191], a[232:235], v[100:103], a[176:191]
ds_read_b64_tr_b16 a[220:221], v6 offset:28672
ds_read_b64_tr_b16 a[222:223], v6 offset:29184
v_mfma_f32_32x32x16_bf16 a[192:207], a[236:239], v[100:103], a[192:207]
ds_read_b128 a[80:83], v13 offset:24576
ds_read_b128 a[84:87], v13 offset:28672
s_waitcnt lgkmcnt(6)
v_mfma_f32_32x32x16_bf16 a[144:159], a[208:211], v[104:107], a[144:159]
ds_read_b64_tr_b16 a[224:225], v1 offset:22528
ds_read_b64_tr_b16 a[226:227], v1 offset:23040
ds_read_b64_tr_b16 a[228:229], v4 offset:22528
ds_read_b64_tr_b16 a[230:231], v4 offset:23040
s_add_u32 m0, s84, 0x2000
s_mov_b32 s87, 0x70000
buffer_load_dwordx4 v11, s[44:47], s87 offen lds
v_mfma_f32_32x32x16_bf16 a[160:175], a[212:215], v[104:107], a[160:175]
ds_read_b64_tr_b16 a[232:233], v3 offset:30720
ds_read_b64_tr_b16 a[234:235], v3 offset:31232
ds_read_b64_tr_b16 a[236:237], v6 offset:30720
ds_read_b64_tr_b16 a[238:239], v6 offset:31232
s_waitcnt lgkmcnt(10)
v_mfma_f32_32x32x16_bf16 a[176:191], a[216:219], v[104:107], a[176:191]
buffer_load_dwordx4 v12, s[44:47], s87 offen offset:1024 lds
v_mfma_f32_32x32x16_bf16 a[192:207], a[220:223], v[104:107], a[192:207]
s_waitcnt lgkmcnt(0)
v_mfma_f32_32x32x16_bf16 a[144:159], a[224:227], v[108:111], a[144:159]
s_waitcnt vmcnt(29)
s_barrier
ds_read_b64_tr_b16 a[208:209], v1 offset:32768
ds_read_b64_tr_b16 a[210:211], v1 offset:33280
ds_read_b64_tr_b16 a[212:213], v4 offset:32768
v_mfma_f32_32x32x16_bf16 a[160:175], a[228:231], v[108:111], a[160:175]
ds_read_b64_tr_b16 a[214:215], v4 offset:33280
ds_read_b64_tr_b16 a[216:217], v2 offset:0
ds_read_b64_tr_b16 a[218:219], v2 offset:512
s_add_u32 m0, s84, 0x26000
s_mov_b32 s87, 0x8c000
buffer_load_dwordx4 v11, s[44:47], s87 offen lds
v_mfma_f32_32x32x16_bf16 a[176:191], a[232:235], v[108:111], a[176:191]
ds_read_b64_tr_b16 a[220:221], v5 offset:0
ds_read_b64_tr_b16 a[222:223], v5 offset:512
buffer_load_dwordx4 v12, s[44:47], s87 offen offset:1024 lds
v_mfma_f32_32x32x16_bf16 a[192:207], a[236:239], v[108:111], a[192:207]
s_waitcnt lgkmcnt(4)
v_mfma_f32_32x32x16_bf16 a[144:159], a[208:211], v[112:115], a[144:159]
ds_read_b64_tr_b16 a[224:225], v1 offset:34816
ds_read_b64_tr_b16 a[226:227], v1 offset:35328
ds_read_b64_tr_b16 a[228:229], v4 offset:34816
v_mfma_f32_32x32x16_bf16 a[160:175], a[212:215], v[112:115], a[160:175]
ds_read_b64_tr_b16 a[230:231], v4 offset:35328
ds_read_b64_tr_b16 a[232:233], v2 offset:2048
ds_read_b64_tr_b16 a[234:235], v2 offset:2560
s_add_u32 m0, s84, 0x6000
s_mul_i32 s85, s75, 0x38000
buffer_load_dwordx4 v7, s[28:31], s85 offen lds
s_waitcnt lgkmcnt(6)
v_mfma_f32_32x32x16_bf16 a[176:191], a[216:219], v[112:115], a[176:191]
ds_read_b64_tr_b16 a[236:237], v5 offset:2048
ds_read_b64_tr_b16 a[238:239], v5 offset:2560
buffer_load_dwordx4 v8, s[28:31], s85 offen offset:1024 lds
v_mfma_f32_32x32x16_bf16 a[192:207], a[220:223], v[112:115], a[192:207]
s_waitcnt lgkmcnt(4)
v_mfma_f32_32x32x16_bf16 a[144:159], a[224:227], v[116:119], a[144:159]
ds_read_b64_tr_b16 a[208:209], v1 offset:36864
ds_read_b64_tr_b16 a[210:211], v1 offset:37376
ds_read_b64_tr_b16 a[212:213], v4 offset:36864
v_mfma_f32_32x32x16_bf16 a[160:175], a[228:231], v[116:119], a[160:175]
ds_read_b64_tr_b16 a[214:215], v4 offset:37376
ds_read_b64_tr_b16 a[216:217], v2 offset:4096
ds_read_b64_tr_b16 a[218:219], v2 offset:4608
s_waitcnt lgkmcnt(6)
v_mfma_f32_32x32x16_bf16 a[176:191], a[232:235], v[116:119], a[176:191]
ds_read_b64_tr_b16 a[220:221], v5 offset:4096
ds_read_b64_tr_b16 a[222:223], v5 offset:4608
s_add_u32 m0, s84, 0x4000
s_nop 0
buffer_load_dwordx4 v7, s[32:35], s85 offen lds
v_mfma_f32_32x32x16_bf16 a[192:207], a[236:239], v[116:119], a[192:207]
ds_read_b128 a[88:91], v13 offset:57344
ds_read_b128 a[92:95], v13 offset:61440
s_waitcnt lgkmcnt(6)
v_mfma_f32_32x32x16_bf16 a[144:159], a[208:211], v[120:123], a[144:159]
ds_read_b64_tr_b16 a[224:225], v1 offset:38912
ds_read_b64_tr_b16 a[226:227], v1 offset:39424
ds_read_b64_tr_b16 a[228:229], v4 offset:38912
ds_read_b64_tr_b16 a[230:231], v4 offset:39424
s_waitcnt vmcnt(8)
v_add_u32 v252, v129, v131
s_waitcnt vmcnt(7)
v_add3_u32 v252, v252, v133, v135
v_cmp_ne_u32 vcc, v252, v248
s_nop 1
s_cbranch_vccnz .Lg_retry
.Lg_ok:
v_fma_f32 v214, v232, v132, v128
buffer_load_dwordx4 v8, s[32:35], s85 offen offset:1024 lds
v_mfma_f32_32x32x16_bf16 a[160:175], a[212:215], v[120:123], a[160:175]
ds_read_b64_tr_b16 a[232:233], v2 offset:6144
ds_read_b64_tr_b16 a[234:235], v2 offset:6656
ds_read_b64_tr_b16 a[236:237], v5 offset:6144
ds_read_b64_tr_b16 a[238:239], v5 offset:6656
v_fma_f32 v215, v232, v134, v130
v_fma_f32 v214, -v233, v134, v214
v_fma_f32 v215, v233, v132, v215
v_mul_f32 v212, v230, v214
v_mul_f32 v213, v230, v215
s_waitcnt lgkmcnt(10)
v_mfma_f32_32x32x16_bf16 a[176:191], a[216:219], v[120:123], a[176:191]
v_fma_f32 v212, -v231, v215, v212
v_fma_f32 v213, v231, v214, v213
v_mov_b32 v214, v212
v_mov_b32 v215, v213
s_nop 0
v_permlane32_swap_b32 v212, v214
v_permlane32_swap_b32 v213, v215
v_add_f32 v212, v212, v214
v_add_f32 v213, v213, v215
v_and_b32 v249, 0xfffffcff, v239
ds_write_b64 v239, v[212:213] offset:2048
v_mfma_f32_32x32x16_bf16 a[192:207], a[220:223], v[120:123], a[192:207]
s_waitcnt lgkmcnt(1)
v_mfma_f32_32x32x16_bf16 a[144:159], a[224:227], v[124:127], a[144:159]
s_waitcnt lgkmcnt(0)
s_barrier
ds_read_b64_tr_b16 a[208:209], v1 offset:49152
ds_read_b64_tr_b16 a[210:211], v1 offset:49664
ds_read_b64_tr_b16 a[212:213], v4 offset:49152
s_add_u32 m0, s84, 0xe000
s_mov_b32 s87, 0xa8000
buffer_load_dwordx4 v11, s[44:47], s87 offen lds
v_mfma_f32_32x32x16_bf16 a[160:175], a[228:231], v[124:127], a[160:175]
ds_read_b64_tr_b16 a[214:215], v4 offset:49664
ds_read_b64_tr_b16 a[216:217], v2 offset:24576
ds_read_b64_tr_b16 a[218:219], v2 offset:25088
ds_read_b64 v[216:217], v249 offset:2048
buffer_load_dwordx4 v12, s[44:47], s87 offen offset:1024 lds
v_mfma_f32_32x32x16_bf16 a[176:191], a[232:235], v[124:127], a[176:191]
ds_read_b64_tr_b16 a[220:221], v5 offset:24576
ds_read_b64_tr_b16 a[222:223], v5 offset:25088
ds_read_b64 v[218:219], v249 offset:2304
ds_read_b64 v[220:221], v249 offset:2560
ds_read_b64 v[222:223], v249 offset:2816
v_mfma_f32_32x32x16_bf16 a[192:207], a[236:239], v[124:127], a[192:207]
s_add_u32 m0, s84, 0x8000
s_mov_b32 s87, 0xc4000
buffer_load_dwordx4 v11, s[44:47], s87 offen lds
s_waitcnt lgkmcnt(8)
v_mfma_f32_32x32x16_bf16 a[112:127], a[208:211], v[16:19], 0
ds_read_b64_tr_b16 a[224:225], v1 offset:51200
ds_read_b64_tr_b16 a[226:227], v1 offset:51712
ds_read_b64_tr_b16 a[228:229], v4 offset:51200
s_waitcnt lgkmcnt(5)
v_add_f32 v216, v216, v218
v_add_f32 v217, v217, v219
buffer_load_dwordx4 v12, s[44:47], s87 offen offset:1024 lds
v_mfma_f32_32x32x16_bf16 a[128:143], a[212:215], v[16:19], 0
ds_read_b64_tr_b16 a[230:231], v4 offset:51712
ds_read_b64_tr_b16 a[232:233], v2 offset:26624
ds_read_b64_tr_b16 a[234:235], v2 offset:27136
s_waitcnt lgkmcnt(6)
v_add_f32 v220, v220, v222
v_mfma_f32_32x32x16_bf16 v[128:143], a[216:219], v[16:19], 0
ds_read_b64_tr_b16 a[236:237], v5 offset:26624
ds_read_b64_tr_b16 a[238:239], v5 offset:27136
v_add_f32 v221, v221, v223
v_add_f32 v212, v216, v220
v_add_f32 v213, v217, v221
v_accvgpr_read_b32 v252, a144
v_accvgpr_read_b32 v253, a145
v_accvgpr_read_b32 v254, a176
v_accvgpr_read_b32 v255, a177
v_mul_f32 v252, v252, v254
v_mul_f32 v253, v253, v255
v_cvt_pk_bf16_f32 v228, v252, v253
v_accvgpr_read_b32 v252, a146
v_mfma_f32_32x32x16_bf16 v[144:159], a[220:223], v[16:19], 0
v_fma_f32 v208, v234, v212, v210
v_fma_f32 v209, v234, v213, v211
v_fma_f32 v208, -v235, v213, v208
v_fma_f32 v209, v235, v212, v209
v_accvgpr_read_b32 v253, a147
v_accvgpr_read_b32 v254, a178
v_accvgpr_read_b32 v255, a179
v_mul_f32 v252, v252, v254
v_mul_f32 v253, v253, v255
v_cvt_pk_bf16_f32 v229, v252, v253
v_accvgpr_read_b32 v252, a148
v_accvgpr_read_b32 v253, a149
s_waitcnt lgkmcnt(4)
v_mfma_f32_32x32x16_bf16 a[112:127], a[224:227], v[20:23], a[112:127]
ds_read_b64_tr_b16 a[208:209], v1 offset:53248
ds_read_b64_tr_b16 a[210:211], v1 offset:53760
ds_read_b64_tr_b16 a[212:213], v4 offset:53248
v_bfe_u32 v250, %2, 5, 1
v_lshlrev_b32 v250, 9, v250
v_sub_u32 v250, s84, v250
v_accvgpr_read_b32 v254, a180
v_accvgpr_read_b32 v255, a181
v_mul_f32 v252, v252, v254
v_mul_f32 v253, v253, v255
v_cvt_pk_bf16_f32 v230, v252, v253
v_accvgpr_read_b32 v252, a150
v_accvgpr_read_b32 v253, a151
v_mfma_f32_32x32x16_bf16 a[128:143], a[228:231], v[20:23], a[128:143]
ds_read_b64_tr_b16 a[214:215], v4 offset:53760
ds_read_b64_tr_b16 a[216:217], v2 offset:28672
ds_read_b64_tr_b16 a[218:219], v2 offset:29184
v_add_u32 v232, v1, v250
v_accvgpr_read_b32 v254, a182
v_accvgpr_read_b32 v255, a183
v_mul_f32 v252, v252, v254
v_mul_f32 v253, v253, v255
v_cvt_pk_bf16_f32 v231, v252, v253
buffer_store_dwordx4 v[228:231], v238, s[48:51], 0 offen sc1
v_accvgpr_read_b32 v252, a152
v_accvgpr_read_b32 v253, a153
s_waitcnt lgkmcnt(6)
v_mfma_f32_32x32x16_bf16 v[128:143], a[232:235], v[20:23], v[128:143]
ds_read_b64_tr_b16 a[220:221], v5 offset:28672
ds_read_b64_tr_b16 a[222:223], v5 offset:29184
v_add_u32 v233, v2, v250
v_add_u32 v234, v3, v250
v_add_u32 v235, v4, v250
v_accvgpr_read_b32 v254, a184
v_accvgpr_read_b32 v255, a185
v_mul_f32 v252, v252, v254
v_mul_f32 v253, v253, v255
v_cvt_pk_bf16_f32 v216, v252, v253
v_accvgpr_read_b32 v252, a154
v_accvgpr_read_b32 v253, a155
v_accvgpr_read_b32 v254, a186
v_mfma_f32_32x32x16_bf16 v[144:159], a[236:239], v[20:23], v[144:159]
ds_read_b128 a[96:99], v14 offset:16384
ds_read_b128 a[100:103], v14 offset:20480
v_add_u32 v248, v5, v250
v_add_u32 v9, v6, v250
v_accvgpr_read_b32 v255, a187
v_mul_f32 v252, v252, v254
v_mul_f32 v253, v253, v255
v_cvt_pk_bf16_f32 v217, v252, v253
v_accvgpr_read_b32 v252, a156
v_accvgpr_read_b32 v253, a157
v_accvgpr_read_b32 v254, a188
v_accvgpr_read_b32 v255, a189
s_waitcnt lgkmcnt(6)
v_mfma_f32_32x32x16_bf16 a[112:127], a[208:211], v[24:27], a[112:127]
ds_read_b64_tr_b16 a[224:225], v1 offset:55296
ds_read_b64_tr_b16 a[226:227], v1 offset:55808
ds_read_b64_tr_b16 a[228:229], v4 offset:55296
ds_read_b64_tr_b16 a[230:231], v4 offset:55808
v_mul_f32 v252, v252, v254
v_mul_f32 v253, v253, v255
v_cvt_pk_bf16_f32 v218, v252, v253
v_accvgpr_read_b32 v252, a158
v_accvgpr_read_b32 v253, a159
v_accvgpr_read_b32 v254, a190
v_accvgpr_read_b32 v255, a191
v_mul_f32 v252, v252, v254
v_mfma_f32_32x32x16_bf16 a[128:143], a[212:215], v[24:27], a[128:143]
ds_read_b64_tr_b16 a[232:233], v2 offset:30720
ds_read_b64_tr_b16 a[234:235], v2 offset:31232
ds_read_b64_tr_b16 a[236:237], v5 offset:30720
ds_read_b64_tr_b16 a[238:239], v5 offset:31232
v_mul_f32 v253, v253, v255
v_cvt_pk_bf16_f32 v219, v252, v253
buffer_store_dwordx4 v[216:219], v238, s[48:51], 0 offen offset:1024 sc1
v_accvgpr_read_b32 v252, a160
v_accvgpr_read_b32 v253, a161
v_accvgpr_read_b32 v254, a192
v_accvgpr_read_b32 v255, a193
v_mul_f32 v252, v252, v254
s_waitcnt lgkmcnt(10)
v_mfma_f32_32x32x16_bf16 v[128:143], a[216:219], v[24:27], v[128:143]
ds_read_b64_tr_b16 a[240:241], v233 offset:8192
ds_read_b64_tr_b16 a[242:243], v233 offset:9216
v_mul_f32 v253, v253, v255
v_cvt_pk_bf16_f32 v228, v252, v253
v_accvgpr_read_b32 v252, a162
v_accvgpr_read_b32 v253, a163
v_accvgpr_read_b32 v254, a194
v_accvgpr_read_b32 v255, a195
v_mul_f32 v252, v252, v254
v_mul_f32 v253, v253, v255
v_cvt_pk_bf16_f32 v229, v252, v253
v_accvgpr_read_b32 v252, a164
v_mfma_f32_32x32x16_bf16 v[144:159], a[220:223], v[24:27], v[144:159]
ds_read_b64_tr_b16 a[244:245], v248 offset:8192
ds_read_b64_tr_b16 a[246:247], v248 offset:9216
v_accvgpr_read_b32 v253, a165
v_accvgpr_read_b32 v254, a196
v_accvgpr_read_b32 v255, a197
v_mul_f32 v252, v252, v254
v_mul_f32 v253, v253, v255
v_cvt_pk_bf16_f32 v230, v252, v253
v_accvgpr_read_b32 v252, a166
v_accvgpr_read_b32 v253, a167
v_accvgpr_read_b32 v254, a198
v_accvgpr_read_b32 v255, a199
s_waitcnt lgkmcnt(4)
v_mfma_f32_32x32x16_bf16 a[112:127], a[224:227], v[28:31], a[112:127]
s_barrier
ds_read_b64_tr_b16 a[208:209], v2 offset:49152
ds_read_b64_tr_b16 a[210:211], v2 offset:49664
ds_read_b64_tr_b16 a[212:213], v5 offset:49152
v_mul_f32 v252, v252, v254
v_mul_f32 v253, v253, v255
v_cvt_pk_bf16_f32 v231, v252, v253
buffer_store_dwordx4 v[228:231], v238, s[48:51], 0 offen offset:2048 sc1
v_accvgpr_read_b32 v252, a168
v_accvgpr_read_b32 v253, a169
v_accvgpr_read_b32 v254, a200
v_mfma_f32_32x32x16_bf16 a[128:143], a[228:231], v[28:31], a[128:143]
ds_read_b64_tr_b16 a[214:215], v5 offset:49664
ds_read_b64_tr_b16 a[216:217], v2 offset:40960
ds_read_b64_tr_b16 a[218:219], v2 offset:41472
v_accvgpr_read_b32 v255, a201
v_mul_f32 v252, v252, v254
v_mul_f32 v253, v253, v255
v_cvt_pk_bf16_f32 v216, v252, v253
v_accvgpr_read_b32 v252, a170
v_accvgpr_read_b32 v253, a171
v_accvgpr_read_b32 v254, a202
v_accvgpr_read_b32 v255, a203
v_mul_f32 v252, v252, v254
s_add_u32 m0, s84, 0x16000
s_mul_i32 s85, s76, 0x38000
buffer_load_dwordx4 v7, s[28:31], s85 offen lds
v_mfma_f32_32x32x16_bf16 v[128:143], a[232:235], v[28:31], v[128:143]
ds_read_b64_tr_b16 a[220:221], v5 offset:40960
ds_read_b64_tr_b16 a[222:223], v5 offset:41472
ds_read_b64_tr_b16 a[248:249], v234 offset:0
ds_read_b64_tr_b16 a[250:251], v234 offset:1024
v_mul_f32 v253, v253, v255
v_cvt_pk_bf16_f32 v217, v252, v253
v_accvgpr_read_b32 v252, a172
v_accvgpr_read_b32 v253, a173
v_accvgpr_read_b32 v254, a204
v_accvgpr_read_b32 v255, a205
v_mul_f32 v252, v252, v254
v_mul_f32 v253, v253, v255
buffer_load_dwordx4 v8, s[28:31], s85 offen offset:1024 lds
v_mfma_f32_32x32x16_bf16 v[144:159], a[236:239], v[28:31], v[144:159]
ds_read_b64_tr_b16 a[252:253], v9 offset:0
ds_read_b64_tr_b16 a[254:255], v9 offset:1024
v_cvt_pk_bf16_f32 v218, v252, v253
v_accvgpr_read_b32 v252, a174
v_accvgpr_read_b32 v253, a175
v_accvgpr_read_b32 v254, a206
v_accvgpr_read_b32 v255, a207
v_mul_f32 v252, v252, v254
v_mul_f32 v253, v253, v255
v_cvt_pk_bf16_f32 v219, v252, v253
buffer_store_dwordx4 v[216:219], v238, s[48:51], 0 offen offset:3072 sc1
s_waitcnt lgkmcnt(8)
v_mfma_f32_32x32x16_bf16 a[112:127], a[208:211], v[32:35], a[112:127]
ds_read_b64_tr_b16 a[224:225], v2 offset:51200
ds_read_b64_tr_b16 a[226:227], v2 offset:51712
ds_read_b64_tr_b16 a[228:229], v5 offset:51200
v_and_b32 v216, 31, %2
v_bfe_u32 v217, %2, 5, 1
v_bfe_u32 v218, %2, 1, 3
v_xor_b32 v218, v218, v217
v_lshlrev_b32 v218, 3, v218
v_lshl_add_u32 v218, v216, 6, v218
v_add_u32 v239, s84, v218
s_add_u32 m0, s84, 0x14000
s_nop 0
buffer_load_dwordx4 v7, s[32:35], s85 offen lds
v_mfma_f32_32x32x16_bf16 a[128:143], a[212:215], v[32:35], a[128:143]
ds_read_b64_tr_b16 a[230:231], v5 offset:51712
ds_read_b64_tr_b16 a[232:233], v2 offset:43008
ds_read_b64_tr_b16 a[234:235], v2 offset:43520
v_bfe_u32 v216, %2, 2, 2
v_lshl_add_u32 v216, v217, 3, v216
v_bfe_u32 v217, %2, 4, 1
v_and_b32 v218, 3, %2
v_lshl_or_b32 v217, v217, 2, v218
v_lshrrev_b32 v218, 1, v216
buffer_load_dwordx4 v8, s[32:35], s85 offen offset:1024 lds
s_waitcnt lgkmcnt(10)
v_mfma_f32_32x32x16_bf16 v[128:143], a[216:219], v[32:35], v[128:143]
ds_read_b64_tr_b16 a[236:237], v5 offset:43008
ds_read_b64_tr_b16 a[238:239], v5 offset:43520
ds_read_b64_tr_b16 v[16:17], v233 offset:57344
ds_read_b64_tr_b16 v[18:19], v233 offset:58368
v_xor_b32 v217, v217, v218
v_lshlrev_b32 v217, 3, v217
v_lshl_add_u32 v217, v216, 6, v217
v_add_u32 v240, s84, v217
v_fma_f32 v212, v226, v208, v192
v_mfma_f32_32x32x16_bf16 v[144:159], a[220:223], v[32:35], v[144:159]
ds_read_b64_tr_b16 v[20:21], v248 offset:57344
ds_read_b64_tr_b16 v[22:23], v248 offset:58368
v_fma_f32 v213, v226, v209, v193
v_fma_f32 v212, -v227, v209, v212
v_fma_f32 v213, v227, v208, v213
v_cndmask_b32 v252, v208, v212, s[12:13]
v_cndmask_b32 v253, v209, v213, s[12:13]
v_fma_f32 v210, v226, v212, v194
s_add_u32 m0, s84, 0xc000
s_mov_b32 s87, 0xe0000
buffer_load_dwordx4 v11, s[44:47], s87 offen lds
s_waitcnt lgkmcnt(8)
v_mfma_f32_32x32x16_bf16 a[112:127], a[224:227], v[36:39], a[112:127]
ds_read_b64_tr_b16 a[208:209], v2 offset:53248
ds_read_b64_tr_b16 a[210:211], v2 offset:53760
ds_read_b64_tr_b16 a[212:213], v5 offset:53248
v_fma_f32 v211, v226, v213, v195
v_fma_f32 v210, -v227, v213, v210
v_fma_f32 v211, v227, v212, v211
v_fma_f32 v254, v224, v252, v160
v_fma_f32 v255, v224, v253, v176
v_fma_f32 v254, -v225, v253, v254
v_fma_f32 v255, v225, v252, v255
buffer_load_dwordx4 v12, s[44:47], s87 offen offset:1024 lds
v_mfma_f32_32x32x16_bf16 a[128:143], a[228:231], v[36:39], a[128:143]
ds_read_b64_tr_b16 a[214:215], v5 offset:53760
ds_read_b64_tr_b16 a[216:217], v2 offset:45056
ds_read_b64_tr_b16 a[218:219], v2 offset:45568
v_mul_f32 v160, v236, v254
v_mul_f32 v176, v236, v255
v_fma_f32 v160, -v237, v255, v160
v_fma_f32 v176, v237, v254, v176
v_fma_f32 v252, v224, v254, v161
v_fma_f32 v253, v224, v255, v177
s_add_u32 m0, s84, 0x12000
s_mov_b32 s87, 0xfc000
buffer_load_dwordx4 v11, s[44:47], s87 offen lds
s_waitcnt lgkmcnt(10)
v_mfma_f32_32x32x16_bf16 v[128:143], a[232:235], v[36:39], v[128:143]
ds_read_b64_tr_b16 a[220:221], v5 offset:45056
ds_read_b64_tr_b16 a[222:223], v5 offset:45568
v_fma_f32 v252, -v225, v255, v252
v_fma_f32 v253, v225, v254, v253
v_mul_f32 v161, v236, v252
v_mul_f32 v177, v236, v253
v_fma_f32 v161, -v237, v253, v161
v_fma_f32 v177, v237, v252, v177
v_fma_f32 v254, v224, v252, v162
v_fma_f32 v255, v224, v253, v178
buffer_load_dwordx4 v12, s[44:47], s87 offen offset:1024 lds
v_mfma_f32_32x32x16_bf16 v[144:159], a[236:239], v[36:39], v[144:159]
ds_read_b128 a[104:107], v14 offset:32768
ds_read_b128 a[108:111], v14 offset:36864
v_fma_f32 v254, -v225, v253, v254
v_fma_f32 v255, v225, v252, v255
v_mul_f32 v162, v236, v254
v_mul_f32 v178, v236, v255
v_fma_f32 v162, -v237, v255, v162
v_fma_f32 v178, v237, v254, v178
s_waitcnt lgkmcnt(6)
v_mfma_f32_32x32x16_bf16 a[112:127], a[208:211], v[40:43], a[112:127]
ds_read_b64_tr_b16 a[224:225], v2 offset:55296
ds_read_b64_tr_b16 a[226:227], v2 offset:55808
ds_read_b64_tr_b16 a[228:229], v5 offset:55296
ds_read_b64_tr_b16 a[230:231], v5 offset:55808
v_fma_f32 v252, v224, v254, v163
v_fma_f32 v253, v224, v255, v179
v_fma_f32 v252, -v225, v255, v252
v_fma_f32 v253, v225, v254, v253
v_mul_f32 v163, v236, v252
v_mul_f32 v179, v236, v253
s_add_u32 m0, s84, 0x20000
s_mov_b32 s87, 0x118000
buffer_load_dwordx4 v11, s[44:47], s87 offen lds
v_mfma_f32_32x32x16_bf16 a[128:143], a[212:215], v[40:43], a[128:143]
ds_read_b64_tr_b16 a[232:233], v2 offset:47104
ds_read_b64_tr_b16 a[234:235], v2 offset:47616
ds_read_b64_tr_b16 a[236:237], v5 offset:47104
ds_read_b64_tr_b16 a[238:239], v5 offset:47616
v_fma_f32 v163, -v237, v253, v163
v_fma_f32 v179, v237, v252, v179
v_xor_b32 v241, 0, v239
v_cvt_pk_bf16_f32 v216, v160, v161
v_cvt_pk_bf16_f32 v217, v162, v163
buffer_load_dwordx4 v12, s[44:47], s87 offen offset:1024 lds
s_waitcnt lgkmcnt(10)
v_mfma_f32_32x32x16_bf16 v[128:143], a[216:219], v[40:43], v[128:143]
ds_read_b64_tr_b16 v[24:25], v232 offset:0
ds_read_b64_tr_b16 v[26:27], v232 offset:1024
v_add_u32 v249, 0x10000, v241
v_add_u32 v241, 0xa000, v241
v_cvt_pk_bf16_f32 v218, -v176, -v177
v_cvt_pk_bf16_f32 v219, -v178, -v179
ds_write_b64 v241, v[216:217] offset:0
ds_write_b64 v249, v[218:219] offset:0
s_add_u32 m0, s84, 0x1e000
s_mov_b32 s87, 0x134000
buffer_load_dwordx4 v11, s[44:47], s87 offen lds
v_mfma_f32_32x32x16_bf16 v[144:159], a[220:223], v[40:43], v[144:159]
ds_read_b64_tr_b16 v[28:29], v235 offset:0
ds_read_b64_tr_b16 v[30:31], v235 offset:1024
v_fma_f32 v212, v226, v210, v196
v_fma_f32 v213, v226, v211, v197
v_fma_f32 v212, -v227, v211, v212
v_fma_f32 v213, v227, v210, v213
v_cndmask_b32 v252, v210, v212, s[12:13]
v_cndmask_b32 v253, v211, v213, s[12:13]
v_fma_f32 v208, v226, v212, v198
buffer_load_dwordx4 v12, s[44:47], s87 offen offset:1024 lds
s_waitcnt lgkmcnt(6)
v_mfma_f32_32x32x16_bf16 a[112:127], a[224:227], v[44:47], a[112:127]
s_barrier
ds_read_b64_tr_b16 a[208:209], v3 offset:16384
ds_read_b64_tr_b16 a[210:211], v3 offset:16896
ds_read_b64_tr_b16 a[212:213], v6 offset:16384
v_fma_f32 v209, v226, v213, v199
v_fma_f32 v208, -v227, v213, v208
v_fma_f32 v209, v227, v212, v209
v_fma_f32 v254, v224, v252, v164
v_mfma_f32_32x32x16_bf16 a[128:143], a[228:231], v[44:47], a[128:143]
ds_read_b64_tr_b16 a[214:215], v6 offset:16896
ds_read_b64_tr_b16 a[216:217], v3 offset:8192
ds_read_b64_tr_b16 a[218:219], v3 offset:8704
v_fma_f32 v255, v224, v253, v180
v_fma_f32 v254, -v225, v253, v254
v_fma_f32 v255, v225, v252, v255
v_mul_f32 v164, v236, v254
v_mul_f32 v180, v236, v255
v_fma_f32 v164, -v237, v255, v164
s_add_u32 m0, s84, 0x18000
s_mul_i32 s85, s77, 0x38000
buffer_load_dwordx4 v7, s[28:31], s85 offen lds
v_mfma_f32_32x32x16_bf16 v[128:143], a[232:235], v[44:47], v[128:143]
ds_read_b64_tr_b16 a[220:221], v6 offset:8192
ds_read_b64_tr_b16 a[222:223], v6 offset:8704
v_fma_f32 v180, v237, v254, v180
v_fma_f32 v252, v224, v254, v165
v_fma_f32 v253, v224, v255, v181
v_fma_f32 v252, -v225, v255, v252
v_fma_f32 v253, v225, v254, v253
v_mul_f32 v165, v236, v252
v_mul_f32 v181, v236, v253
buffer_load_dwordx4 v8, s[28:31], s85 offen offset:1024 lds
v_mfma_f32_32x32x16_bf16 v[144:159], a[236:239], v[44:47], v[144:159]
v_fma_f32 v165, -v237, v253, v165
v_fma_f32 v181, v237, v252, v181
v_fma_f32 v254, v224, v252, v166
v_fma_f32 v255, v224, v253, v182
v_fma_f32 v254, -v225, v253, v254
v_fma_f32 v255, v225, v252, v255
v_mul_f32 v166, v236, v254
v_mul_f32 v182, v236, v255
v_fma_f32 v166, -v237, v255, v166
s_add_u32 m0, s84, 0x1a000
s_nop 0
buffer_load_dwordx4 v7, s[32:35], s85 offen lds
s_waitcnt lgkmcnt(4)
v_mfma_f32_32x32x16_bf16 a[112:127], a[208:211], v[48:51], a[112:127]
ds_read_b64_tr_b16 a[224:225], v3 offset:18432
ds_read_b64_tr_b16 a[226:227], v3 offset:18944
ds_read_b64_tr_b16 a[228:229], v6 offset:18432
s_mov_b32 s91, 0
s_waitcnt vmcnt(13)
buffer_store_dwordx2 v[244:245], v242, s[56:59], s91 offen sc1
v_fma_f32 v182, v237, v254, v182
v_fma_f32 v252, v224, v254, v167
v_fma_f32 v253, v224, v255, v183
v_fma_f32 v252, -v225, v255, v252
buffer_load_dwordx4 v8, s[32:35], s85 offen offset:1024 lds
v_mfma_f32_32x32x16_bf16 a[128:143], a[212:215], v[48:51], a[128:143]
ds_read_b64_tr_b16 a[230:231], v6 offset:18944
ds_read_b64_tr_b16 a[232:233], v3 offset:10240
ds_read_b64_tr_b16 a[234:235], v3 offset:10752
v_fma_f32 v253, v225, v254, v253
v_mul_f32 v167, v236, v252
v_mul_f32 v183, v236, v253
v_fma_f32 v167, -v237, v253, v167
v_fma_f32 v183, v237, v252, v183
s_waitcnt lgkmcnt(6)
v_mfma_f32_32x32x16_bf16 v[128:143], a[216:219], v[48:51], v[128:143]
ds_read_b64_tr_b16 a[236:237], v6 offset:10240
ds_read_b64_tr_b16 a[238:239], v6 offset:10752
ds_read_b64_tr_b16 v[32:33], v232 offset:8192
ds_read_b64_tr_b16 v[34:35], v232 offset:9216
v_xor_b32 v241, 16, v239
v_cvt_pk_bf16_f32 v216, v164, v165
v_cvt_pk_bf16_f32 v217, v166, v167
v_add_u32 v249, 0x10000, v241
v_add_u32 v241, 0xa000, v241
v_mfma_f32_32x32x16_bf16 v[144:159], a[220:223], v[48:51], v[144:159]
ds_read_b64_tr_b16 v[36:37], v235 offset:8192
ds_read_b64_tr_b16 v[38:39], v235 offset:9216
v_cvt_pk_bf16_f32 v218, -v180, -v181
v_cvt_pk_bf16_f32 v219, -v182, -v183
ds_write_b64 v241, v[216:217] offset:0
ds_write_b64 v249, v[218:219] offset:0
v_fma_f32 v212, v226, v208, v200
v_fma_f32 v213, v226, v209, v201
v_fma_f32 v212, -v227, v209, v212
s_add_u32 m0, s84, 0x1c000
s_mul_i32 s85, s78, 0x38000
buffer_load_dwordx4 v7, s[28:31], s85 offen lds
s_waitcnt lgkmcnt(10)
v_mfma_f32_32x32x16_bf16 a[112:127], a[224:227], v[52:55], a[112:127]
ds_read_b64_tr_b16 a[208:209], v3 offset:20480
ds_read_b64_tr_b16 a[210:211], v3 offset:20992
ds_read_b64_tr_b16 a[212:213], v6 offset:20480
v_fma_f32 v213, v227, v208, v213
v_cndmask_b32 v252, v208, v212, s[12:13]
v_cndmask_b32 v253, v209, v213, s[12:13]
v_fma_f32 v210, v226, v212, v202
v_fma_f32 v211, v226, v213, v203
v_fma_f32 v210, -v227, v213, v210
v_fma_f32 v211, v227, v212, v211
buffer_load_dwordx4 v8, s[28:31], s85 offen offset:1024 lds
v_mfma_f32_32x32x16_bf16 a[128:143], a[228:231], v[52:55], a[128:143]
ds_read_b64_tr_b16 a[214:215], v6 offset:20992
ds_read_b64_tr_b16 a[216:217], v3 offset:12288
ds_read_b64_tr_b16 a[218:219], v3 offset:12800
v_fma_f32 v254, v224, v252, v168
v_fma_f32 v255, v224, v253, v184
v_fma_f32 v254, -v225, v253, v254
v_fma_f32 v255, v225, v252, v255
v_mul_f32 v168, v236, v254
s_waitcnt lgkmcnt(12)
v_mfma_f32_32x32x16_bf16 v[128:143], a[232:235], v[52:55], v[128:143]
ds_read_b64_tr_b16 a[220:221], v6 offset:12288
ds_read_b64_tr_b16 a[222:223], v6 offset:12800
v_mul_f32 v184, v236, v255
v_fma_f32 v168, -v237, v255, v168
v_fma_f32 v184, v237, v254, v184
v_fma_f32 v252, v224, v254, v169
v_fma_f32 v253, v224, v255, v185
v_fma_f32 v252, -v225, v255, v252
v_fma_f32 v253, v225, v254, v253
v_mul_f32 v169, v236, v252
s_add_u32 m0, s84, 0
s_mov_b32 s87, 0x150000
buffer_load_dwordx4 v11, s[44:47], s87 offen lds
v_mfma_f32_32x32x16_bf16 v[144:159], a[236:239], v[52:55], v[144:159]
v_mul_f32 v185, v236, v253
v_fma_f32 v169, -v237, v253, v169
v_fma_f32 v185, v237, v252, v185
v_fma_f32 v254, v224, v252, v170
v_fma_f32 v255, v224, v253, v186
v_fma_f32 v254, -v225, v253, v254
v_fma_f32 v255, v225, v252, v255
v_mul_f32 v170, v236, v254
v_mul_f32 v186, v236, v255
buffer_load_dwordx4 v12, s[44:47], s87 offen offset:1024 lds
s_waitcnt lgkmcnt(4)
v_mfma_f32_32x32x16_bf16 a[112:127], a[208:211], v[56:59], a[112:127]
ds_read_b64_tr_b16 a[224:225], v3 offset:22528
ds_read_b64_tr_b16 a[226:227], v3 offset:23040
ds_read_b64_tr_b16 a[228:229], v6 offset:22528
ds_read_b64_tr_b16 a[230:231], v6 offset:23040
v_fma_f32 v170, -v237, v255, v170
v_fma_f32 v186, v237, v254, v186
v_fma_f32 v252, v224, v254, v171
v_fma_f32 v253, v224, v255, v187
v_fma_f32 v252, -v225, v255, v252
v_fma_f32 v253, v225, v254, v253
s_add_u32 m0, s84, 0x2000
s_mov_b32 s87, 0x16c000
buffer_load_dwordx4 v11, s[44:47], s87 offen lds
v_mfma_f32_32x32x16_bf16 a[128:143], a[212:215], v[56:59], a[128:143]
ds_read_b64_tr_b16 a[232:233], v3 offset:14336
ds_read_b64_tr_b16 a[234:235], v3 offset:14848
ds_read_b64_tr_b16 a[236:237], v6 offset:14336
ds_read_b64_tr_b16 a[238:239], v6 offset:14848
v_mul_f32 v171, v236, v252
v_mul_f32 v187, v236, v253
v_fma_f32 v171, -v237, v253, v171
v_fma_f32 v187, v237, v252, v187
buffer_load_dwordx4 v12, s[44:47], s87 offen offset:1024 lds
s_waitcnt lgkmcnt(8)
v_mfma_f32_32x32x16_bf16 v[128:143], a[216:219], v[56:59], v[128:143]
ds_read_b64_tr_b16 v[40:41], v234 offset:24576
ds_read_b64_tr_b16 v[42:43], v234 offset:25600
v_xor_b32 v241, 32, v239
v_cvt_pk_bf16_f32 v216, v168, v169
v_cvt_pk_bf16_f32 v217, v170, v171
v_add_u32 v249, 0x10000, v241
v_add_u32 v241, 0xa000, v241
v_cvt_pk_bf16_f32 v218, -v184, -v185
v_cvt_pk_bf16_f32 v219, -v186, -v187
v_mfma_f32_32x32x16_bf16 v[144:159], a[220:223], v[56:59], v[144:159]
ds_read_b64_tr_b16 v[44:45], v9 offset:24576
ds_read_b64_tr_b16 v[46:47], v9 offset:25600
ds_write_b64 v241, v[216:217] offset:0
ds_write_b64 v249, v[218:219] offset:0
v_fma_f32 v212, v226, v210, v204
v_fma_f32 v213, v226, v211, v205
v_fma_f32 v212, -v227, v211, v212
v_fma_f32 v213, v227, v210, v213
v_cndmask_b32 v252, v210, v212, s[12:13]
s_waitcnt lgkmcnt(6)
v_mfma_f32_32x32x16_bf16 a[112:127], a[224:227], v[60:63], a[112:127]
s_barrier
ds_read_b64_tr_b16 a[208:209], v1 offset:24576
ds_read_b64_tr_b16 a[210:211], v1 offset:25088
ds_read_b64_tr_b16 a[212:213], v4 offset:24576
v_cndmask_b32 v253, v211, v213, s[12:13]
v_fma_f32 v254, v224, v252, v172
v_fma_f32 v255, v224, v253, v188
s_add_u32 m0, s84, 0x22000
s_nop 0
buffer_load_dwordx4 v7, s[32:35], s85 offen lds
v_mfma_f32_32x32x16_bf16 a[128:143], a[228:231], v[60:63], a[128:143]
ds_read_b64_tr_b16 a[214:215], v4 offset:25088
ds_read_b64_tr_b16 a[216:217], v1 offset:16384
ds_read_b64_tr_b16 a[218:219], v1 offset:16896
v_fma_f32 v254, -v225, v253, v254
v_fma_f32 v255, v225, v252, v255
v_mul_f32 v172, v236, v254
v_mul_f32 v188, v236, v255
v_fma_f32 v172, -v237, v255, v172
v_fma_f32 v188, v237, v254, v188
buffer_load_dwordx4 v8, s[32:35], s85 offen offset:1024 lds
v_mfma_f32_32x32x16_bf16 v[128:143], a[232:235], v[60:63], v[128:143]
ds_read_b64_tr_b16 a[220:221], v4 offset:16384
ds_read_b64_tr_b16 a[222:223], v4 offset:16896
v_fma_f32 v252, v224, v254, v173
v_fma_f32 v253, v224, v255, v189
v_fma_f32 v252, -v225, v255, v252
v_fma_f32 v253, v225, v254, v253
v_mul_f32 v173, v236, v252
v_mul_f32 v189, v236, v253
v_fma_f32 v173, -v237, v253, v173
v_fma_f32 v189, v237, v252, v189
s_add_u32 m0, s84, 0x24000
s_mov_b32 s87, 0xe000
buffer_load_dwordx4 v11, s[44:47], s87 offen lds
v_mfma_f32_32x32x16_bf16 v[144:159], a[236:239], v[60:63], v[144:159]
v_fma_f32 v254, v224, v252, v174
v_fma_f32 v255, v224, v253, v190
v_fma_f32 v254, -v225, v253, v254
v_fma_f32 v255, v225, v252, v255
v_mul_f32 v174, v236, v254
v_mul_f32 v190, v236, v255
v_fma_f32 v174, -v237, v255, v174
v_fma_f32 v190, v237, v254, v190
v_fma_f32 v252, v224, v254, v175
s_waitcnt lgkmcnt(4)
v_mfma_f32_32x32x16_bf16 a[112:127], a[208:211], v[64:67], a[112:127]
ds_read_b64_tr_b16 a[224:225], v1 offset:26624
ds_read_b64_tr_b16 a[226:227], v1 offset:27136
ds_read_b64_tr_b16 a[228:229], v4 offset:26624
v_fma_f32 v253, v224, v255, v191
v_fma_f32 v252, -v225, v255, v252
v_fma_f32 v253, v225, v254, v253
v_mul_f32 v175, v236, v252
v_mul_f32 v191, v236, v253
v_fma_f32 v175, -v237, v253, v175
buffer_load_dwordx4 v12, s[44:47], s87 offen offset:1024 lds
v_mfma_f32_32x32x16_bf16 a[128:143], a[212:215], v[64:67], a[128:143]
ds_read_b64_tr_b16 a[230:231], v4 offset:27136
ds_read_b64_tr_b16 a[232:233], v1 offset:18432
ds_read_b64_tr_b16 a[234:235], v1 offset:18944
v_fma_f32 v191, v237, v252, v191
v_xor_b32 v241, 48, v239
v_cvt_pk_bf16_f32 v216, v172, v173
v_cvt_pk_bf16_f32 v217, v174, v175
v_add_u32 v249, 0x10000, v241
v_add_u32 v241, 0xa000, v241
s_add_u32 m0, s84, 0x26000
s_mov_b32 s87, 0x2a000
buffer_load_dwordx4 v11, s[44:47], s87 offen lds
s_waitcnt lgkmcnt(6)
v_mfma_f32_32x32x16_bf16 v[128:143], a[216:219], v[64:67], v[128:143]
ds_read_b64_tr_b16 a[236:237], v4 offset:18432
ds_read_b64_tr_b16 a[238:239], v4 offset:18944
ds_read_b64_tr_b16 v[48:49], v232 offset:57344
ds_read_b64_tr_b16 v[50:51], v232 offset:58368
v_cvt_pk_bf16_f32 v218, -v188, -v189
v_cvt_pk_bf16_f32 v219, -v190, -v191
ds_write_b64 v241, v[216:217] offset:0
ds_write_b64 v249, v[218:219] offset:0
v_xor_b32 v253, 16, v240
buffer_load_dwordx4 v12, s[44:47], s87 offen offset:1024 lds
v_mfma_f32_32x32x16_bf16 v[144:159], a[220:223], v[64:67], v[144:159]
ds_read_b64_tr_b16 v[52:53], v235 offset:57344
ds_read_b64_tr_b16 v[54:55], v235 offset:58368
v_add_u32 v252, 0xa000, v240
v_add_u32 v254, 0x10000, v240
v_add_u32 v255, 0x10100, v253
v_add_u32 v253, 0xa100, v253
s_lshl_b32 s10, s6, 12
s_sub_u32 s10, 0x1c000, s10
s_waitcnt lgkmcnt(10)
v_mfma_f32_32x32x16_bf16 a[112:127], a[224:227], v[68:71], a[112:127]
ds_read_b64_tr_b16 a[208:209], v1 offset:28672
ds_read_b64_tr_b16 a[210:211], v1 offset:29184
ds_read_b64_tr_b16 a[212:213], v4 offset:28672
v_mfma_f32_32x32x16_bf16 a[128:143], a[228:231], v[68:71], a[128:143]
ds_read_b64_tr_b16 a[214:215], v4 offset:29184
ds_read_b64_tr_b16 a[216:217], v1 offset:20480
ds_read_b64_tr_b16 a[218:219], v1 offset:20992
s_waitcnt lgkmcnt(8)
ds_read_b64_tr_b16 v[160:161], v252 offset:0
v_mfma_f32_32x32x16_bf16 v[128:143], a[232:235], v[68:71], v[128:143]
ds_read_b64_tr_b16 a[220:221], v4 offset:20480
ds_read_b64_tr_b16 a[222:223], v4 offset:20992
ds_read_b64_tr_b16 v[162:163], v253 offset:0
ds_read_b64_tr_b16 v[164:165], v252 offset:1024
ds_read_b64_tr_b16 v[166:167], v253 offset:1024
v_mfma_f32_32x32x16_bf16 v[144:159], a[236:239], v[68:71], v[144:159]
ds_read_b64_tr_b16 v[168:169], v254 offset:0
ds_read_b64_tr_b16 v[170:171], v255 offset:0
ds_read_b64_tr_b16 v[172:173], v254 offset:1024
ds_read_b64_tr_b16 v[174:175], v255 offset:1024
s_waitcnt lgkmcnt(12)
v_mfma_f32_32x32x16_bf16 a[112:127], a[208:211], v[72:75], a[112:127]
ds_read_b64_tr_b16 a[224:225], v1 offset:30720
ds_read_b64_tr_b16 a[226:227], v1 offset:31232
ds_read_b64_tr_b16 a[228:229], v4 offset:30720
ds_read_b64_tr_b16 a[230:231], v4 offset:31232
s_add_u32 m0, s84, 0xe000
s_mov_b32 s87, 0x46000
buffer_load_dwordx4 v11, s[44:47], s87 offen lds
v_mfma_f32_32x32x16_bf16 a[128:143], a[212:215], v[72:75], a[128:143]
ds_read_b64_tr_b16 a[232:233], v1 offset:22528
ds_read_b64_tr_b16 a[234:235], v1 offset:23040
ds_read_b64_tr_b16 a[236:237], v4 offset:22528
ds_read_b64_tr_b16 a[238:239], v4 offset:23040
buffer_load_dwordx4 v12, s[44:47], s87 offen offset:1024 lds
s_waitcnt lgkmcnt(15)
v_mfma_f32_32x32x16_bf16 v[128:143], a[216:219], v[72:75], v[128:143]
ds_read_b64_tr_b16 v[56:57], v232 offset:32768
ds_read_b64_tr_b16 v[58:59], v232 offset:33792
s_waitcnt lgkmcnt(15)
buffer_store_dwordx4 v[160:163], v238, s[48:51], s10 offen sc1
s_waitcnt lgkmcnt(14)
buffer_store_dwordx4 v[164:167], v238, s[48:51], s10 offen offset:1024 sc1
s_waitcnt lgkmcnt(12)
buffer_store_dwordx4 v[168:171], v238, s[48:51], s10 offen offset:2048 sc1
s_waitcnt lgkmcnt(10)
buffer_store_dwordx4 v[172:175], v238, s[48:51], s10 offen offset:3072 sc1
v_mfma_f32_32x32x16_bf16 v[144:159], a[220:223], v[72:75], v[144:159]
ds_read_b64_tr_b16 v[60:61], v235 offset:32768
ds_read_b64_tr_b16 v[62:63], v235 offset:33792
s_waitcnt lgkmcnt(4)
v_mfma_f32_32x32x16_bf16 a[112:127], a[224:227], v[76:79], a[112:127]
s_waitcnt vmcnt(31)
s_barrier
ds_read_b64_tr_b16 a[208:209], v2 offset:24576
ds_read_b64_tr_b16 a[210:211], v2 offset:25088
ds_read_b64_tr_b16 a[212:213], v5 offset:24576
v_mfma_f32_32x32x16_bf16 a[128:143], a[228:231], v[76:79], a[128:143]
ds_read_b64_tr_b16 a[214:215], v5 offset:25088
ds_read_b64_tr_b16 a[216:217], v2 offset:16384
ds_read_b64_tr_b16 a[218:219], v2 offset:16896
v_mfma_f32_32x32x16_bf16 v[128:143], a[232:235], v[76:79], v[128:143]
ds_read_b64_tr_b16 a[220:221], v5 offset:16384
ds_read_b64_tr_b16 a[222:223], v5 offset:16896
v_mfma_f32_32x32x16_bf16 v[144:159], a[236:239], v[76:79], v[144:159]
s_waitcnt lgkmcnt(4)
v_mfma_f32_32x32x16_bf16 a[112:127], a[208:211], v[80:83], a[112:127]
ds_read_b64_tr_b16 a[224:225], v2 offset:26624
ds_read_b64_tr_b16 a[226:227], v2 offset:27136
ds_read_b64_tr_b16 a[228:229], v5 offset:26624
v_mfma_f32_32x32x16_bf16 a[128:143], a[212:215], v[80:83], a[128:143]
ds_read_b64_tr_b16 a[230:231], v5 offset:27136
ds_read_b64_tr_b16 a[232:233], v2 offset:18432
ds_read_b64_tr_b16 a[234:235], v2 offset:18944
s_waitcnt lgkmcnt(6)
v_mfma_f32_32x32x16_bf16 v[128:143], a[216:219], v[80:83], v[128:143]
ds_read_b64_tr_b16 a[236:237], v5 offset:18432
ds_read_b64_tr_b16 a[238:239], v5 offset:18944
s_waitcnt vmcnt(29)
ds_read_b64_tr_b16 v[64:65], v232 offset:49152
ds_read_b64_tr_b16 v[66:67], v232 offset:50176
v_mfma_f32_32x32x16_bf16 v[144:159], a[220:223], v[80:83], v[144:159]
ds_read_b64_tr_b16 v[68:69], v235 offset:49152
ds_read_b64_tr_b16 v[70:71], v235 offset:50176
s_waitcnt lgkmcnt(8)
v_mfma_f32_32x32x16_bf16 a[112:127], a[224:227], v[84:87], a[112:127]
ds_read_b64_tr_b16 a[208:209], v2 offset:28672
ds_read_b64_tr_b16 a[210:211], v2 offset:29184
ds_read_b64_tr_b16 a[212:213], v5 offset:28672
s_mov_b32 s91, 0
buffer_load_dwordx2 v[250:251], v243, s[56:59], s91 offen sc1
v_mfma_f32_32x32x16_bf16 a[128:143], a[228:231], v[84:87], a[128:143]
ds_read_b64_tr_b16 a[214:215], v5 offset:29184
ds_read_b64_tr_b16 a[216:217], v2 offset:20480
ds_read_b64_tr_b16 a[218:219], v2 offset:20992
s_waitcnt lgkmcnt(10)
v_mfma_f32_32x32x16_bf16 v[128:143], a[232:235], v[84:87], v[128:143]
ds_read_b64_tr_b16 a[220:221], v5 offset:20480
ds_read_b64_tr_b16 a[222:223], v5 offset:20992
v_mfma_f32_32x32x16_bf16 v[144:159], a[236:239], v[84:87], v[144:159]
s_waitcnt lgkmcnt(4)
v_mfma_f32_32x32x16_bf16 a[112:127], a[208:211], v[88:91], a[112:127]
ds_read_b64_tr_b16 a[224:225], v2 offset:30720
ds_read_b64_tr_b16 a[226:227], v2 offset:31232
ds_read_b64_tr_b16 a[228:229], v5 offset:30720
ds_read_b64_tr_b16 a[230:231], v5 offset:31232
v_mfma_f32_32x32x16_bf16 a[128:143], a[212:215], v[88:91], a[128:143]
ds_read_b64_tr_b16 a[232:233], v2 offset:22528
ds_read_b64_tr_b16 a[234:235], v2 offset:23040
ds_read_b64_tr_b16 a[236:237], v5 offset:22528
ds_read_b64_tr_b16 a[238:239], v5 offset:23040
s_waitcnt lgkmcnt(8)
v_mfma_f32_32x32x16_bf16 v[128:143], a[216:219], v[88:91], v[128:143]
s_waitcnt vmcnt(28)
ds_read_b64_tr_b16 v[72:73], v233 offset:8192
ds_read_b64_tr_b16 v[74:75], v233 offset:9216
v_mfma_f32_32x32x16_bf16 v[144:159], a[220:223], v[88:91], v[144:159]
ds_read_b64_tr_b16 v[76:77], v248 offset:8192
ds_read_b64_tr_b16 v[78:79], v248 offset:9216
s_waitcnt lgkmcnt(4)
v_mfma_f32_32x32x16_bf16 a[112:127], a[224:227], v[92:95], a[112:127]
s_waitcnt vmcnt(19)
s_barrier
ds_read_b64_tr_b16 a[208:209], v2 offset:32768
ds_read_b64_tr_b16 a[210:211], v2 offset:33280
ds_read_b64_tr_b16 a[212:213], v5 offset:32768
s_add_u32 m0, s84, 0x10000
s_mov_b32 s87, 0x62000
buffer_load_dwordx4 v11, s[44:47], s87 offen lds
v_mfma_f32_32x32x16_bf16 a[128:143], a[228:231], v[92:95], a[128:143]
ds_read_b64_tr_b16 a[214:215], v5 offset:33280
ds_read_b64_tr_b16 a[216:217], v2 offset:40960
ds_read_b64_tr_b16 a[218:219], v2 offset:41472
buffer_load_dwordx4 v12, s[44:47], s87 offen offset:1024 lds
v_mfma_f32_32x32x16_bf16 v[128:143], a[232:235], v[92:95], v[128:143]
ds_read_b64_tr_b16 a[220:221], v5 offset:40960
ds_read_b64_tr_b16 a[222:223], v5 offset:41472
v_mfma_f32_32x32x16_bf16 v[144:159], a[236:239], v[92:95], v[144:159]
s_add_u32 m0, s84, 0x14000
s_mov_b32 s87, 0x7e000
buffer_load_dwordx4 v11, s[44:47], s87 offen lds
s_waitcnt lgkmcnt(4)
v_mfma_f32_32x32x16_bf16 a[112:127], a[208:211], v[96:99], a[112:127]
ds_read_b64_tr_b16 a[224:225], v2 offset:34816
ds_read_b64_tr_b16 a[226:227], v2 offset:35328
ds_read_b64_tr_b16 a[228:229], v5 offset:34816
buffer_load_dwordx4 v12, s[44:47], s87 offen offset:1024 lds
v_mfma_f32_32x32x16_bf16 a[128:143], a[212:215], v[96:99], a[128:143]
ds_read_b64_tr_b16 a[230:231], v5 offset:35328
ds_read_b64_tr_b16 a[232:233], v2 offset:43008
ds_read_b64_tr_b16 a[234:235], v2 offset:43520
s_waitcnt lgkmcnt(6)
v_mfma_f32_32x32x16_bf16 v[128:143], a[216:219], v[96:99], v[128:143]
ds_read_b64_tr_b16 a[236:237], v5 offset:43008
ds_read_b64_tr_b16 a[238:239], v5 offset:43520
v_mfma_f32_32x32x16_bf16 v[144:159], a[220:223], v[96:99], v[144:159]
s_waitcnt lgkmcnt(4)
v_mfma_f32_32x32x16_bf16 a[112:127], a[224:227], v[100:103], a[112:127]
ds_read_b64_tr_b16 a[208:209], v2 offset:36864
ds_read_b64_tr_b16 a[210:211], v2 offset:37376
ds_read_b64_tr_b16 a[212:213], v5 offset:36864
s_add_u32 m0, s84, 0x16000
s_mov_b32 s87, 0x9a000
buffer_load_dwordx4 v11, s[44:47], s87 offen lds
v_mfma_f32_32x32x16_bf16 a[128:143], a[228:231], v[100:103], a[128:143]
ds_read_b64_tr_b16 a[214:215], v5 offset:37376
ds_read_b64_tr_b16 a[216:217], v2 offset:45056
ds_read_b64_tr_b16 a[218:219], v2 offset:45568
s_waitcnt lgkmcnt(6)
v_mfma_f32_32x32x16_bf16 v[128:143], a[232:235], v[100:103], v[128:143]
ds_read_b64_tr_b16 a[220:221], v5 offset:45056
ds_read_b64_tr_b16 a[222:223], v5 offset:45568
s_mul_i32 s11, s7, 0x150
buffer_load_dwordx4 v12, s[44:47], s87 offen offset:1024 lds
v_mfma_f32_32x32x16_bf16 v[144:159], a[236:239], v[100:103], v[144:159]
s_add_u32 s11, s11, s4
s_lshl_b32 s11, s11, 10
s_waitcnt lgkmcnt(4)
v_mfma_f32_32x32x16_bf16 a[112:127], a[208:211], v[104:107], a[112:127]
ds_read_b64_tr_b16 a[224:225], v2 offset:38912
ds_read_b64_tr_b16 a[226:227], v2 offset:39424
ds_read_b64_tr_b16 a[228:229], v5 offset:38912
ds_read_b64_tr_b16 a[230:231], v5 offset:39424
v_mfma_f32_32x32x16_bf16 a[128:143], a[212:215], v[104:107], a[128:143]
ds_read_b64_tr_b16 a[232:233], v2 offset:47104
ds_read_b64_tr_b16 a[234:235], v2 offset:47616
ds_read_b64_tr_b16 a[236:237], v5 offset:47104
ds_read_b64_tr_b16 a[238:239], v5 offset:47616
s_add_u32 m0, s84, 0x12000
s_mov_b32 s87, 0xb6000
buffer_load_dwordx4 v11, s[44:47], s87 offen lds
s_waitcnt lgkmcnt(8)
v_mfma_f32_32x32x16_bf16 v[128:143], a[216:219], v[104:107], v[128:143]
v_and_b32 v7, 63, %2
v_lshlrev_b32 v7, 4, v7
v_add_u32 v7, s11, v7
buffer_load_dwordx4 v12, s[44:47], s87 offen offset:1024 lds
v_mfma_f32_32x32x16_bf16 v[144:159], a[220:223], v[104:107], v[144:159]
v_add_u32 v8, 0x2a000, v7
s_waitcnt lgkmcnt(0)
v_mfma_f32_32x32x16_bf16 a[112:127], a[224:227], v[108:111], a[112:127]
s_waitcnt vmcnt(19)
s_barrier
ds_read_b64_tr_b16 a[208:209], v2 offset:49152
ds_read_b64_tr_b16 a[210:211], v2 offset:49664
ds_read_b64_tr_b16 a[212:213], v5 offset:49152
v_mfma_f32_32x32x16_bf16 a[128:143], a[228:231], v[108:111], a[128:143]
ds_read_b64_tr_b16 a[214:215], v5 offset:49664
ds_read_b64_tr_b16 a[216:217], v3 offset:8192
ds_read_b64_tr_b16 a[218:219], v3 offset:8704
s_add_u32 m0, s84, 0x18000
s_mov_b32 s87, 0xd2000
buffer_load_dwordx4 v11, s[44:47], s87 offen lds
v_mfma_f32_32x32x16_bf16 v[128:143], a[232:235], v[108:111], v[128:143]
ds_read_b64_tr_b16 a[220:221], v6 offset:8192
ds_read_b64_tr_b16 a[222:223], v6 offset:8704
buffer_load_dwordx4 v12, s[44:47], s87 offen offset:1024 lds
v_mfma_f32_32x32x16_bf16 v[144:159], a[236:239], v[108:111], v[144:159]
s_waitcnt lgkmcnt(4)
v_mfma_f32_32x32x16_bf16 a[112:127], a[208:211], v[112:115], a[112:127]
ds_read_b64_tr_b16 a[224:225], v2 offset:51200
ds_read_b64_tr_b16 a[226:227], v2 offset:51712
ds_read_b64_tr_b16 a[228:229], v5 offset:51200
s_waitcnt vmcnt(10)
v_cmp_ne_u64 vcc, v[250:251], v[246:247]
s_nop 1
s_cbranch_vccnz .La_retry
.La_ok:
s_mov_b32 s91, 0x20000
buffer_store_dwordx2 v[244:245], v242, s[56:59], s91 offen sc1
v_mfma_f32_32x32x16_bf16 a[128:143], a[212:215], v[112:115], a[128:143]
ds_read_b64_tr_b16 a[230:231], v5 offset:51712
ds_read_b64_tr_b16 a[232:233], v3 offset:10240
ds_read_b64_tr_b16 a[234:235], v3 offset:10752
s_mov_b32 s88, 0
buffer_load_dwordx4 v[160:163], v7, s[48:51], s88 offen sc1
s_waitcnt lgkmcnt(6)
v_mfma_f32_32x32x16_bf16 v[128:143], a[216:219], v[112:115], v[128:143]
ds_read_b64_tr_b16 a[236:237], v6 offset:10240
ds_read_b64_tr_b16 a[238:239], v6 offset:10752
buffer_load_dwordx4 v[164:167], v8, s[48:51], s88 offen sc1
v_mfma_f32_32x32x16_bf16 v[144:159], a[220:223], v[112:115], v[144:159]
s_mov_b32 s88, 0x2000
buffer_load_dwordx4 v[168:171], v7, s[48:51], s88 offen sc1
s_waitcnt lgkmcnt(4)
v_mfma_f32_32x32x16_bf16 a[112:127], a[224:227], v[116:119], a[112:127]
ds_read_b64_tr_b16 a[208:209], v2 offset:53248
ds_read_b64_tr_b16 a[210:211], v2 offset:53760
ds_read_b64_tr_b16 a[212:213], v5 offset:53248
buffer_load_dwordx4 v[172:175], v8, s[48:51], s88 offen sc1
v_mfma_f32_32x32x16_bf16 a[128:143], a[228:231], v[116:119], a[128:143]
ds_read_b64_tr_b16 a[214:215], v5 offset:53760
ds_read_b64_tr_b16 a[216:217], v3 offset:12288
ds_read_b64_tr_b16 a[218:219], v3 offset:12800
s_mov_b32 s88, 0x4000
buffer_load_dwordx4 v[176:179], v7, s[48:51], s88 offen sc1
s_waitcnt lgkmcnt(6)
v_mfma_f32_32x32x16_bf16 v[128:143], a[232:235], v[116:119], v[128:143]
ds_read_b64_tr_b16 a[220:221], v6 offset:12288
ds_read_b64_tr_b16 a[222:223], v6 offset:12800
buffer_load_dwordx4 v[180:183], v8, s[48:51], s88 offen sc1
v_mfma_f32_32x32x16_bf16 v[144:159], a[236:239], v[116:119], v[144:159]
s_mov_b32 s88, 0x6000
buffer_load_dwordx4 v[184:187], v7, s[48:51], s88 offen sc1
s_waitcnt lgkmcnt(4)
v_mfma_f32_32x32x16_bf16 a[112:127], a[208:211], v[120:123], a[112:127]
ds_read_b64_tr_b16 a[224:225], v2 offset:55296
ds_read_b64_tr_b16 a[226:227], v2 offset:55808
ds_read_b64_tr_b16 a[228:229], v5 offset:55296
ds_read_b64_tr_b16 a[230:231], v5 offset:55808
buffer_load_dwordx4 v[188:191], v8, s[48:51], s88 offen sc1
v_mfma_f32_32x32x16_bf16 a[128:143], a[212:215], v[120:123], a[128:143]
ds_read_b64_tr_b16 a[232:233], v3 offset:14336
ds_read_b64_tr_b16 a[234:235], v3 offset:14848
ds_read_b64_tr_b16 a[236:237], v6 offset:14336
ds_read_b64_tr_b16 a[238:239], v6 offset:14848
s_mov_b32 s88, 0x8000
buffer_load_dwordx4 v[192:195], v7, s[48:51], s88 offen sc1
s_waitcnt lgkmcnt(8)
v_mfma_f32_32x32x16_bf16 v[128:143], a[216:219], v[120:123], v[128:143]
buffer_load_dwordx4 v[196:199], v8, s[48:51], s88 offen sc1
v_mfma_f32_32x32x16_bf16 v[144:159], a[220:223], v[120:123], v[144:159]
s_mov_b32 s88, 0xa000
buffer_load_dwordx4 v[200:203], v7, s[48:51], s88 offen sc1
s_waitcnt lgkmcnt(4)
v_mfma_f32_32x32x16_bf16 a[112:127], a[224:227], v[124:127], a[112:127]
buffer_load_dwordx4 v[204:207], v8, s[48:51], s88 offen sc1
v_mfma_f32_32x32x16_bf16 a[128:143], a[228:231], v[124:127], a[128:143]
s_waitcnt lgkmcnt(0)
v_mfma_f32_32x32x16_bf16 v[128:143], a[232:235], v[124:127], v[128:143]
s_mov_b32 s88, 0xc000
buffer_load_dwordx4 v[208:211], v7, s[48:51], s88 offen sc1
v_mfma_f32_32x32x16_bf16 v[144:159], a[236:239], v[124:127], v[144:159]
s_waitcnt vmcnt(11)
v_mfma_f32_32x32x16_bf16 a[144:159], a[240:243], v[160:163], 0
buffer_load_dwordx4 v[212:215], v8, s[48:51], s88 offen sc1
v_mfma_f32_32x32x16_bf16 a[160:175], a[240:243], v[164:167], 0
s_mov_b32 s11, 0x1000
v_accvgpr_read_b32 v252, a112
v_accvgpr_read_b32 v253, a113
s_nop 1
v_mul_f32 v252, v252, v128
v_mul_f32 v253, v253, v129
v_cvt_pk_bf16_f32 v128, v252, v253
v_accvgpr_read_b32 v254, a114
v_mfma_f32_32x32x16_bf16 a[176:191], a[244:247], v[160:163], 0
v_accvgpr_read_b32 v255, a115
v_mul_f32 v254, v254, v130
v_mul_f32 v255, v255, v131
v_cvt_pk_bf16_f32 v129, v254, v255
v_accvgpr_read_b32 v252, a116
v_accvgpr_read_b32 v253, a117
s_mov_b32 s88, 0xe000
buffer_load_dwordx4 v[216:219], v7, s[48:51], s88 offen sc1
v_mfma_f32_32x32x16_bf16 a[192:207], a[244:247], v[164:167], 0
v_mul_f32 v252, v252, v132
v_mul_f32 v253, v253, v133
v_cvt_pk_bf16_f32 v130, v252, v253
v_accvgpr_read_b32 v254, a118
s_waitcnt vmcnt(11)
v_mfma_f32_32x32x16_bf16 a[144:159], a[248:251], v[168:171], a[144:159]
v_accvgpr_read_b32 v255, a119
v_mul_f32 v254, v254, v134
v_mul_f32 v255, v255, v135
v_cvt_pk_bf16_f32 v131, v254, v255
buffer_store_dwordx4 v[128:131], v238, s[48:51], s11 offen sc1
v_accvgpr_read_b32 v252, a120
v_accvgpr_read_b32 v253, a121
buffer_load_dwordx4 v[220:223], v8, s[48:51], s88 offen sc1
v_mfma_f32_32x32x16_bf16 a[160:175], a[248:251], v[172:175], a[160:175]
v_mul_f32 v252, v252, v136
v_mul_f32 v253, v253, v137
v_cvt_pk_bf16_f32 v136, v252, v253
v_accvgpr_read_b32 v254, a122
v_accvgpr_read_b32 v255, a123
v_mul_f32 v254, v254, v138
v_mfma_f32_32x32x16_bf16 a[176:191], a[252:255], v[168:171], a[176:191]
v_mul_f32 v255, v255, v139
v_cvt_pk_bf16_f32 v137, v254, v255
v_accvgpr_read_b32 v252, a124
v_accvgpr_read_b32 v253, a125
v_mul_f32 v252, v252, v140
v_mul_f32 v253, v253, v141
s_mov_b32 s88, 0x10000
buffer_load_dwordx4 v[80:83], v7, s[48:51], s88 offen sc1
v_mfma_f32_32x32x16_bf16 a[192:207], a[252:255], v[172:175], a[192:207]
v_cvt_pk_bf16_f32 v138, v252, v253
v_accvgpr_read_b32 v254, a126
v_accvgpr_read_b32 v255, a127
v_mul_f32 v254, v254, v142
s_waitcnt vmcnt(12)
v_mfma_f32_32x32x16_bf16 a[144:159], v[16:19], v[176:179], a[144:159]
v_mul_f32 v255, v255, v143
v_cvt_pk_bf16_f32 v139, v254, v255
buffer_store_dwordx4 v[136:139], v238, s[48:51], s11 offen offset:1024 sc1
v_accvgpr_read_b32 v252, a128
v_accvgpr_read_b32 v253, a129
v_mul_f32 v252, v252, v144
v_mul_f32 v253, v253, v145
buffer_load_dwordx4 v[84:87], v8, s[48:51], s88 offen sc1
v_mfma_f32_32x32x16_bf16 a[160:175], v[16:19], v[180:183], a[160:175]
v_cvt_pk_bf16_f32 v144, v252, v253
v_accvgpr_read_b32 v254, a130
v_accvgpr_read_b32 v255, a131
v_mul_f32 v254, v254, v146
v_mul_f32 v255, v255, v147
v_cvt_pk_bf16_f32 v145, v254, v255
v_mfma_f32_32x32x16_bf16 a[176:191], v[20:23], v[176:179], a[176:191]
v_accvgpr_read_b32 v252, a132
v_accvgpr_read_b32 v253, a133
v_mul_f32 v252, v252, v148
v_mul_f32 v253, v253, v149
v_cvt_pk_bf16_f32 v146, v252, v253
v_accvgpr_read_b32 v254, a134
s_mov_b32 s88, 0x12000
buffer_load_dwordx4 v[88:91], v7, s[48:51], s88 offen sc1
v_mfma_f32_32x32x16_bf16 a[192:207], v[20:23], v[180:183], a[192:207]
v_accvgpr_read_b32 v255, a135
v_mul_f32 v254, v254, v150
v_mul_f32 v255, v255, v151
v_cvt_pk_bf16_f32 v147, v254, v255
s_waitcnt vmcnt(13)
v_mfma_f32_32x32x16_bf16 a[144:159], v[24:27], v[184:187], a[144:159]
s_barrier
buffer_store_dwordx4 v[144:147], v238, s[48:51], s11 offen offset:2048 sc1
v_accvgpr_read_b32 v252, a136
v_accvgpr_read_b32 v253, a137
v_mul_f32 v252, v252, v152
v_mul_f32 v253, v253, v153
v_cvt_pk_bf16_f32 v152, v252, v253
buffer_load_dwordx4 v[92:95], v8, s[48:51], s88 offen sc1
v_mfma_f32_32x32x16_bf16 a[160:175], v[24:27], v[188:191], a[160:175]
v_accvgpr_read_b32 v254, a138
v_accvgpr_read_b32 v255, a139
v_mul_f32 v254, v254, v154
v_mul_f32 v255, v255, v155
v_cvt_pk_bf16_f32 v153, v254, v255
v_accvgpr_read_b32 v252, a140
v_mfma_f32_32x32x16_bf16 a[176:191], v[28:31], v[184:187], a[176:191]
v_accvgpr_read_b32 v253, a141
v_mul_f32 v252, v252, v156
v_mul_f32 v253, v253, v157
v_cvt_pk_bf16_f32 v154, v252, v253
v_accvgpr_read_b32 v254, a142
v_accvgpr_read_b32 v255, a143
s_mov_b32 s88, 0x14000
buffer_load_dwordx4 v[96:99], v7, s[48:51], s88 offen sc1
v_mfma_f32_32x32x16_bf16 a[192:207], v[28:31], v[188:191], a[192:207]
v_mul_f32 v254, v254, v158
v_mul_f32 v255, v255, v159
v_cvt_pk_bf16_f32 v155, v254, v255
buffer_store_dwordx4 v[152:155], v238, s[48:51], s11 offen offset:3072 sc1
s_waitcnt vmcnt(15)
v_mfma_f32_32x32x16_bf16 a[144:159], v[32:35], v[192:195], a[144:159]
s_mov_b32 s91, 0x20000
buffer_load_dwordx2 v[250:251], v243, s[56:59], s91 offen sc1
buffer_load_dwordx4 v[100:103], v8, s[48:51], s88 offen sc1
v_mfma_f32_32x32x16_bf16 a[160:175], v[32:35], v[196:199], a[160:175]
v_mfma_f32_32x32x16_bf16 a[176:191], v[36:39], v[192:195], a[176:191]
s_mov_b32 s88, 0x16000
buffer_load_dwordx4 v[104:107], v7, s[48:51], s88 offen sc1
v_mfma_f32_32x32x16_bf16 a[192:207], v[36:39], v[196:199], a[192:207]
s_waitcnt vmcnt(16)
v_mfma_f32_32x32x16_bf16 a[144:159], v[40:43], v[200:203], a[144:159]
buffer_load_dwordx4 v[108:111], v8, s[48:51], s88 offen sc1
v_mfma_f32_32x32x16_bf16 a[160:175], v[40:43], v[204:207], a[160:175]
v_mfma_f32_32x32x16_bf16 a[176:191], v[44:47], v[200:203], a[176:191]
s_mov_b32 s88, 0x18000
buffer_load_dwordx4 v[112:115], v7, s[48:51], s88 offen sc1
v_mfma_f32_32x32x16_bf16 a[192:207], v[44:47], v[204:207], a[192:207]
s_waitcnt vmcnt(16)
v_mfma_f32_32x32x16_bf16 a[144:159], v[48:51], v[208:211], a[144:159]
buffer_load_dwordx4 v[116:119], v8, s[48:51], s88 offen sc1
v_mfma_f32_32x32x16_bf16 a[160:175], v[48:51], v[212:215], a[160:175]
v_mfma_f32_32x32x16_bf16 a[176:191], v[52:55], v[208:211], a[176:191]
s_mov_b32 s88, 0x1a000
buffer_load_dwordx4 v[120:123], v7, s[48:51], s88 offen sc1
v_mfma_f32_32x32x16_bf16 a[192:207], v[52:55], v[212:215], a[192:207]
s_waitcnt vmcnt(15)
v_mfma_f32_32x32x16_bf16 a[144:159], v[56:59], v[216:219], a[144:159]
buffer_load_dwordx4 v[124:127], v8, s[48:51], s88 offen sc1
v_mfma_f32_32x32x16_bf16 a[160:175], v[56:59], v[220:223], a[160:175]
v_mfma_f32_32x32x16_bf16 a[176:191], v[60:63], v[216:219], a[176:191]
v_mfma_f32_32x32x16_bf16 a[192:207], v[60:63], v[220:223], a[192:207]
s_waitcnt vmcnt(13)
v_mfma_f32_32x32x16_bf16 a[144:159], v[64:67], v[80:83], a[144:159]
s_add_u32 m0, s84, 0x1a000
s_mov_b32 s87, 0xee000
buffer_load_dwordx4 v11, s[44:47], s87 offen lds
v_mfma_f32_32x32x16_bf16 a[160:175], v[64:67], v[84:87], a[160:175]
buffer_load_dwordx4 v12, s[44:47], s87 offen offset:1024 lds
v_mfma_f32_32x32x16_bf16 a[176:191], v[68:71], v[80:83], a[176:191]
v_mfma_f32_32x32x16_bf16 a[192:207], v[68:71], v[84:87], a[192:207]
s_add_u32 m0, s84, 0x1c000
s_mov_b32 s87, 0x10a000
buffer_load_dwordx4 v11, s[44:47], s87 offen lds
s_waitcnt vmcnt(13)
v_mfma_f32_32x32x16_bf16 a[144:159], v[72:75], v[88:91], a[144:159]
ds_read_b64_tr_b16 a[240:241], v234 offset:0
ds_read_b64_tr_b16 a[242:243], v234 offset:1024
s_mov_b32 s91, 0x10000
s_waitcnt vmcnt(11)
buffer_store_dwordx2 v[244:245], v242, s[56:59], s91 offen sc1
buffer_load_dwordx4 v12, s[44:47], s87 offen offset:1024 lds
v_mfma_f32_32x32x16_bf16 a[160:175], v[72:75], v[92:95], a[160:175]
ds_read_b64_tr_b16 a[244:245], v9 offset:0
ds_read_b64_tr_b16 a[246:247], v9 offset:1024
v_mfma_f32_32x32x16_bf16 a[176:191], v[76:79], v[88:91], a[176:191]
v_mfma_f32_32x32x16_bf16 a[192:207], v[76:79], v[92:95], a[192:207]
s_waitcnt vmcnt(11)
s_waitcnt lgkmcnt(0)
v_mfma_f32_32x32x16_bf16 a[144:159], a[240:243], v[96:99], a[144:159]
ds_read_b64_tr_b16 a[248:249], v233 offset:57344
ds_read_b64_tr_b16 a[250:251], v233 offset:58368
v_cmp_ne_u64 vcc, v[250:251], v[246:247]
s_nop 1
s_cbranch_vccnz .Lh_retry
.Lh_ok:
s_cmp_eq_u32 s7, 31
s_cselect_b32 s10, s8, 1
s_or_b32 s10, s10, s4
s_cmp_eq_u32 s10, 0
s_cselect_b32 s10, 0, 0x40000000
v_mov_b32 v250, s10
buffer_store_dword v244, v250, s[56:59], 0 offen sc1
v_mfma_f32_32x32x16_bf16 a[160:175], a[240:243], v[100:103], a[160:175]
ds_read_b64_tr_b16 a[252:253], v248 offset:57344
ds_read_b64_tr_b16 a[254:255], v248 offset:58368
v_add_u32 v7, s89, v7
v_add_u32 v8, s89, v8
s_mov_b32 s88, 0x1c000
buffer_load_dwordx4 v[160:163], v7, s[48:51], s88 offen sc1
v_mfma_f32_32x32x16_bf16 a[176:191], a[244:247], v[96:99], a[176:191]
buffer_load_dwordx4 v[164:167], v8, s[48:51], s88 offen sc1
v_mfma_f32_32x32x16_bf16 a[192:207], a[244:247], v[100:103], a[192:207]
s_mov_b32 s88, 0x1e000
buffer_load_dwordx4 v[168:171], v7, s[48:51], s88 offen sc1
s_waitcnt vmcnt(13)
s_waitcnt lgkmcnt(0)
v_mfma_f32_32x32x16_bf16 a[144:159], a[248:251], v[104:107], a[144:159]
ds_read_b64_tr_b16 a[240:241], v232 offset:0
ds_read_b64_tr_b16 a[242:243], v232 offset:1024
buffer_load_dwordx4 v[172:175], v8, s[48:51], s88 offen sc1
v_mfma_f32_32x32x16_bf16 a[160:175], a[248:251], v[108:111], a[160:175]
ds_read_b64_tr_b16 a[244:245], v235 offset:0
ds_read_b64_tr_b16 a[246:247], v235 offset:1024
v_bfe_u32 v154, %2, 3, 3
s_mov_b32 s88, 0x20000
buffer_load_dwordx4 v[176:179], v7, s[48:51], s88 offen sc1
v_mfma_f32_32x32x16_bf16 a[176:191], a[252:255], v[104:107], a[176:191]
v_and_b32 v155, 7, %2
s_lshl_b32 s10, s7, 1
buffer_load_dwordx4 v[180:183], v8, s[48:51], s88 offen sc1
v_mfma_f32_32x32x16_bf16 a[192:207], a[252:255], v[108:111], a[192:207]
s_mov_b32 s88, 0x22000
buffer_load_dwordx4 v[184:187], v7, s[48:51], s88 offen sc1
s_waitcnt vmcnt(15)
s_waitcnt lgkmcnt(0)
v_mfma_f32_32x32x16_bf16 a[144:159], a[240:243], v[112:115], a[144:159]
ds_read_b64_tr_b16 a[248:249], v232 offset:8192
ds_read_b64_tr_b16 a[250:251], v232 offset:9216
buffer_load_dwordx4 v[188:191], v8, s[48:51], s88 offen sc1
v_mfma_f32_32x32x16_bf16 a[160:175], a[240:243], v[116:119], a[160:175]
ds_read_b64_tr_b16 a[252:253], v235 offset:8192
ds_read_b64_tr_b16 a[254:255], v235 offset:9216
s_mov_b32 s88, 0x24000
buffer_load_dwordx4 v[192:195], v7, s[48:51], s88 offen sc1
v_mfma_f32_32x32x16_bf16 a[176:191], a[244:247], v[112:115], a[176:191]
s_and_b32 s11, s4, 1
s_add_u32 s10, s10, s11
buffer_load_dwordx4 v[196:199], v8, s[48:51], s88 offen sc1
v_mfma_f32_32x32x16_bf16 a[192:207], a[244:247], v[116:119], a[192:207]
s_mov_b32 s88, 0x26000
buffer_load_dwordx4 v[200:203], v7, s[48:51], s88 offen sc1
s_waitcnt vmcnt(17)
s_waitcnt lgkmcnt(0)
v_mfma_f32_32x32x16_bf16 a[144:159], a[248:251], v[120:123], a[144:159]
s_lshl_b32 s10, s10, 5
v_add_u32 v152, s10, v154
s_lshl_b32 s10, s8, 1
buffer_load_dwordx4 v[204:207], v8, s[48:51], s88 offen sc1
v_mfma_f32_32x32x16_bf16 a[160:175], a[248:251], v[124:127], a[160:175]
s_lshr_b32 s11, s4, 1
s_add_u32 s10, s10, s11
v_mfma_f32_32x32x16_bf16 a[176:191], a[252:255], v[120:123], a[176:191]
s_lshl_b32 s10, s10, 6
v_lshlrev_b32 v153, 3, v155
s_mov_b32 s88, 0x28000
buffer_load_dwordx4 v[208:211], v7, s[48:51], s88 offen sc1
v_mfma_f32_32x32x16_bf16 a[192:207], a[252:255], v[124:127], a[192:207]
v_add_u32 v153, s10, v153
s_waitcnt vmcnt(11)
v_mfma_f32_32x32x16_bf16 a[112:127], a[0:3], v[160:163], 0
v_mul_u32_u24 v150, 0x380, v152
v_add_u32 v150, v150, v153
buffer_load_dwordx4 v[212:215], v8, s[48:51], s88 offen sc1
v_mfma_f32_32x32x16_bf16 a[128:143], a[0:3], v[164:167], 0
v_lshlrev_b32 v151, 1, v150
s_lshl_b32 s10, s8, 14
v_mfma_f32_32x32x16_bf16 a[208:223], a[4:7], v[160:163], 0
s_add_u32 s10, s10, 0x24000
v_lshlrev_b32 v152, 3, v152
v_add_u32 v152, s10, v152
s_mov_b32 s88, 0x1c800
buffer_load_dwordx4 v[216:219], v7, s[48:51], s88 offen sc1
v_mfma_f32_32x32x16_bf16 a[224:239], a[4:7], v[164:167], 0
s_mov_b32 s11, 0
buffer_load_dwordx2 v[128:129], v150, s[16:19], s11 offen
s_waitcnt vmcnt(12)
v_mfma_f32_32x32x16_bf16 a[112:127], a[8:11], v[168:171], a[112:127]
s_mov_b32 s11, 0x1c00
buffer_load_dwordx2 v[130:131], v150, s[16:19], s11 offen
buffer_load_dwordx4 v[220:223], v8, s[48:51], s88 offen sc1
v_mfma_f32_32x32x16_bf16 a[128:143], a[8:11], v[172:175], a[128:143]
s_mov_b32 s11, 0x3800
buffer_load_dwordx2 v[132:133], v150, s[16:19], s11 offen
v_mfma_f32_32x32x16_bf16 a[208:223], a[12:15], v[168:171], a[208:223]
s_mov_b32 s11, 0x5400
buffer_load_dwordx2 v[134:135], v150, s[16:19], s11 offen
s_mov_b32 s88, 0x1e800
buffer_load_dwordx4 v[80:83], v7, s[48:51], s88 offen sc1
v_mfma_f32_32x32x16_bf16 a[224:239], a[12:15], v[172:175], a[224:239]
s_waitcnt vmcnt(15)
v_mfma_f32_32x32x16_bf16 a[112:127], a[16:19], v[176:179], a[112:127]
global_load_dwordx2 v[144:145], v153, s[66:67]
buffer_load_dwordx2 v[136:137], v152, s[56:59], 0 offen sc1
buffer_load_dwordx2 v[138:139], v152, s[56:59], 0 offen offset:64 sc1
buffer_load_dwordx4 v[84:87], v8, s[48:51], s88 offen sc1
v_mfma_f32_32x32x16_bf16 a[128:143], a[16:19], v[180:183], a[128:143]
buffer_load_dwordx2 v[140:141], v152, s[56:59], 0 offen offset:128 sc1
buffer_load_dwordx2 v[142:143], v152, s[56:59], 0 offen offset:192 sc1
v_mfma_f32_32x32x16_bf16 a[208:223], a[20:23], v[176:179], a[208:223]
v_xor_b32 v13, v155, v154
v_lshlrev_b32 v13, 4, v13
s_mov_b32 s88, 0x20800
buffer_load_dwordx4 v[88:91], v7, s[48:51], s88 offen sc1
v_mfma_f32_32x32x16_bf16 a[224:239], a[20:23], v[180:183], a[224:239]
v_lshl_add_u32 v13, v154, 7, v13
s_waitcnt vmcnt(20)
v_mfma_f32_32x32x16_bf16 a[112:127], a[24:27], v[184:187], a[112:127]
s_mov_b32 s91, 0x10000
buffer_load_dwordx2 v[250:251], v243, s[56:59], s91 offen sc1
buffer_load_dwordx4 v[92:95], v8, s[48:51], s88 offen sc1
v_mfma_f32_32x32x16_bf16 a[128:143], a[24:27], v[188:191], a[128:143]
s_lshl_b32 s10, s4, 14
v_add_u32 v13, s10, v13
v_mfma_f32_32x32x16_bf16 a[208:223], a[28:31], v[184:187], a[208:223]
v_bfe_u32 v154, %2, 5, 1
v_xor_b32 v154, v154, v155
v_lshlrev_b32 v154, 4, v154
s_mov_b32 s88, 0x22800
buffer_load_dwordx4 v[96:99], v7, s[48:51], s88 offen sc1
v_mfma_f32_32x32x16_bf16 a[224:239], a[28:31], v[188:191], a[224:239]
v_and_b32 v155, 15, %2
s_waitcnt vmcnt(21)
v_mfma_f32_32x32x16_bf16 a[112:127], a[32:35], v[192:195], a[112:127]
v_lshl_add_u32 v154, v155, 7, v154
v_bfe_u32 v155, %2, 4, 1
buffer_load_dwordx4 v[100:103], v8, s[48:51], s88 offen sc1
v_mfma_f32_32x32x16_bf16 a[128:143], a[32:35], v[196:199], a[128:143]
v_lshl_add_u32 v154, v155, 13, v154
v_add_u32 v156, s84, v154
v_mfma_f32_32x32x16_bf16 a[208:223], a[36:39], v[192:195], a[208:223]
v_xor_b32 v157, 32, v156
v_xor_b32 v158, 64, v156
s_mov_b32 s88, 0x24800
buffer_load_dwordx4 v[104:107], v7, s[48:51], s88 offen sc1
v_mfma_f32_32x32x16_bf16 a[224:239], a[36:39], v[196:199], a[224:239]
v_xor_b32 v159, 0x60, v156
s_waitcnt vmcnt(21)
v_mfma_f32_32x32x16_bf16 a[112:127], a[40:43], v[200:203], a[112:127]
buffer_load_dwordx4 v[108:111], v8, s[48:51], s88 offen sc1
v_mfma_f32_32x32x16_bf16 a[128:143], a[40:43], v[204:207], a[128:143]
v_mfma_f32_32x32x16_bf16 a[208:223], a[44:47], v[200:203], a[208:223]
s_mov_b32 s88, 0x26800
buffer_load_dwordx4 v[112:115], v7, s[48:51], s88 offen sc1
v_mfma_f32_32x32x16_bf16 a[224:239], a[44:47], v[204:207], a[224:239]
s_waitcnt vmcnt(21)
v_mfma_f32_32x32x16_bf16 a[112:127], a[48:51], v[208:211], a[112:127]
buffer_load_dwordx4 v[116:119], v8, s[48:51], s88 offen sc1
v_mfma_f32_32x32x16_bf16 a[128:143], a[48:51], v[212:215], a[128:143]
v_mfma_f32_32x32x16_bf16 a[208:223], a[52:55], v[208:211], a[208:223]
s_mov_b32 s88, 0x28800
buffer_load_dwordx4 v[120:123], v7, s[48:51], s88 offen sc1
v_mfma_f32_32x32x16_bf16 a[224:239], a[52:55], v[212:215], a[224:239]
s_waitcnt vmcnt(19)
v_mfma_f32_32x32x16_bf16 a[112:127], a[56:59], v[216:219], a[112:127]
s_waitcnt vmcnt(15)
v_lshlrev_b32 v146, 16, v144
v_and_b32 v147, s15, v144
v_lshlrev_b32 v148, 16, v145
v_and_b32 v149, s15, v145
buffer_load_dwordx4 v[124:127], v8, s[48:51], s88 offen sc1
v_mfma_f32_32x32x16_bf16 a[128:143], a[56:59], v[220:223], a[128:143]
v_lshlrev_b32 v16, 16, v128
v_and_b32 v17, s15, v128
s_waitcnt vmcnt(15)
v_sub_f32 v252, v16, v136
v_sub_f32 v253, v17, v136
v_mfma_f32_32x32x16_bf16 a[208:223], a[60:63], v[216:219], a[208:223]
v_mul_f32 v252, v252, v137
v_mul_f32 v253, v253, v137
v_cvt_pk_bf16_f32 v252, v252, v253
v_mfma_f32_32x32x16_bf16 a[224:239], a[60:63], v[220:223], a[224:239]
v_and_b32 v253, s15, v252
v_lshlrev_b32 v252, 16, v252
v_mul_f32 v32, v252, v146
s_add_u32 m0, s84, 0x22000
s_mov_b32 s87, 0x126000
buffer_load_dwordx4 v11, s[44:47], s87 offen lds
s_waitcnt vmcnt(14)
v_mfma_f32_32x32x16_bf16 a[112:127], a[64:67], v[80:83], a[112:127]
s_waitcnt vmcnt(10)
v_cmp_ne_u64 vcc, v[250:251], v[246:247]
s_nop 1
s_cbranch_vccnz .Lb_retry
.Lb_ok:
v_mul_f32 v33, v253, v147
buffer_load_dwordx4 v12, s[44:47], s87 offen offset:1024 lds
v_mfma_f32_32x32x16_bf16 a[128:143], a[64:67], v[84:87], a[128:143]
v_lshlrev_b32 v18, 16, v129
v_and_b32 v19, s15, v129
v_sub_f32 v254, v18, v136
v_sub_f32 v255, v19, v136
v_subrev_u32 v7, s89, v7
v_subrev_u32 v8, s89, v8
s_mov_b32 s88, 0x1000
buffer_load_dwordx4 v[160:163], v7, s[48:51], s88 offen sc1
v_mfma_f32_32x32x16_bf16 a[208:223], a[68:71], v[80:83], a[208:223]
v_mul_f32 v254, v254, v137
v_mul_f32 v255, v255, v137
v_cvt_pk_bf16_f32 v254, v254, v255
buffer_load_dwordx4 v[164:167], v8, s[48:51], s88 offen sc1
v_mfma_f32_32x32x16_bf16 a[224:239], a[68:71], v[84:87], a[224:239]
v_and_b32 v255, s15, v254
v_lshlrev_b32 v254, 16, v254
v_mul_f32 v34, v254, v148
s_mov_b32 s88, 0x3000
buffer_load_dwordx4 v[168:171], v7, s[48:51], s88 offen sc1
s_waitcnt vmcnt(13)
v_mfma_f32_32x32x16_bf16 a[112:127], a[72:75], v[88:91], a[112:127]
v_mul_f32 v35, v255, v149
v_lshlrev_b32 v20, 16, v130
v_and_b32 v21, s15, v130
v_sub_f32 v252, v20, v138
buffer_load_dwordx4 v[172:175], v8, s[48:51], s88 offen sc1
v_mfma_f32_32x32x16_bf16 a[128:143], a[72:75], v[92:95], a[128:143]
v_sub_f32 v253, v21, v138
v_mul_f32 v252, v252, v139
v_mul_f32 v253, v253, v139
v_mfma_f32_32x32x16_bf16 a[208:223], a[76:79], v[88:91], a[208:223]
v_cvt_pk_bf16_f32 v252, v252, v253
v_and_b32 v253, s15, v252
v_lshlrev_b32 v252, 16, v252
v_mul_f32 v36, v252, v146
s_mov_b32 s88, 0x5000
buffer_load_dwordx4 v[176:179], v7, s[48:51], s88 offen sc1
v_mfma_f32_32x32x16_bf16 a[224:239], a[76:79], v[92:95], a[224:239]
v_mul_f32 v37, v253, v147
v_lshlrev_b32 v22, 16, v131
s_waitcnt vmcnt(13)
v_mfma_f32_32x32x16_bf16 a[112:127], a[80:83], v[96:99], a[112:127]
v_and_b32 v23, s15, v131
v_sub_f32 v254, v22, v138
v_sub_f32 v255, v23, v138
v_mul_f32 v254, v254, v139
buffer_load_dwordx4 v[180:183], v8, s[48:51], s88 offen sc1
v_mfma_f32_32x32x16_bf16 a[128:143], a[80:83], v[100:103], a[128:143]
v_mul_f32 v255, v255, v139
v_cvt_pk_bf16_f32 v254, v254, v255
v_and_b32 v255, s15, v254
v_mfma_f32_32x32x16_bf16 a[208:223], a[84:87], v[96:99], a[208:223]
v_lshlrev_b32 v254, 16, v254
v_mul_f32 v38, v254, v148
v_mul_f32 v39, v255, v149
v_lshlrev_b32 v24, 16, v132
s_mov_b32 s88, 0x7000
buffer_load_dwordx4 v[184:187], v7, s[48:51], s88 offen sc1
v_mfma_f32_32x32x16_bf16 a[224:239], a[84:87], v[100:103], a[224:239]
v_and_b32 v25, s15, v132
v_sub_f32 v252, v24, v140
s_waitcnt vmcnt(13)
v_mfma_f32_32x32x16_bf16 a[112:127], a[88:91], v[104:107], a[112:127]
v_sub_f32 v253, v25, v140
v_mul_f32 v252, v252, v141
v_mul_f32 v253, v253, v141
v_cvt_pk_bf16_f32 v252, v252, v253
buffer_load_dwordx4 v[188:191], v8, s[48:51], s88 offen sc1
v_mfma_f32_32x32x16_bf16 a[128:143], a[88:91], v[108:111], a[128:143]
v_and_b32 v253, s15, v252
v_lshlrev_b32 v252, 16, v252
v_mul_f32 v40, v252, v146
v_mul_f32 v41, v253, v147
v_mfma_f32_32x32x16_bf16 a[208:223], a[92:95], v[104:107], a[208:223]
v_lshlrev_b32 v26, 16, v133
v_and_b32 v27, s15, v133
v_sub_f32 v254, v26, v140
s_mov_b32 s88, 0x9000
buffer_load_dwordx4 v[192:195], v7, s[48:51], s88 offen sc1
v_mfma_f32_32x32x16_bf16 a[224:239], a[92:95], v[108:111], a[224:239]
v_sub_f32 v255, v27, v140
v_mul_f32 v254, v254, v141
v_mul_f32 v255, v255, v141
s_waitcnt vmcnt(13)
v_mfma_f32_32x32x16_bf16 a[112:127], a[96:99], v[112:115], a[112:127]
v_cvt_pk_bf16_f32 v254, v254, v255
v_and_b32 v255, s15, v254
v_lshlrev_b32 v254, 16, v254
buffer_load_dwordx4 v[196:199], v8, s[48:51], s88 offen sc1
v_mfma_f32_32x32x16_bf16 a[128:143], a[96:99], v[116:119], a[128:143]
v_mul_f32 v42, v254, v148
v_mul_f32 v43, v255, v149
v_lshlrev_b32 v28, 16, v134
v_and_b32 v29, s15, v134
v_mfma_f32_32x32x16_bf16 a[208:223], a[100:103], v[112:115], a[208:223]
v_sub_f32 v252, v28, v142
v_sub_f32 v253, v29, v142
v_mul_f32 v252, v252, v143
s_mov_b32 s88, 0xb000
buffer_load_dwordx4 v[200:203], v7, s[48:51], s88 offen sc1
v_mfma_f32_32x32x16_bf16 a[224:239], a[100:103], v[116:119], a[224:239]
v_mul_f32 v253, v253, v143
v_cvt_pk_bf16_f32 v252, v252, v253
v_and_b32 v253, s15, v252
s_waitcnt vmcnt(13)
v_mfma_f32_32x32x16_bf16 a[112:127], a[104:107], v[120:123], a[112:127]
ds_read_b64_tr_b16 a[240:241], v234 offset:16384
ds_read_b64_tr_b16 a[242:243], v234 offset:17408
v_lshlrev_b32 v252, 16, v252
buffer_load_dwordx4 v[204:207], v8, s[48:51], s88 offen sc1
v_mfma_f32_32x32x16_bf16 a[128:143], a[104:107], v[124:127], a[128:143]
ds_read_b64_tr_b16 a[244:245], v9 offset:16384
ds_read_b64_tr_b16 a[246:247], v9 offset:17408
v_mul_f32 v44, v252, v146
v_mfma_f32_32x32x16_bf16 a[208:223], a[108:111], v[120:123], a[208:223]
v_mul_f32 v45, v253, v147
v_lshlrev_b32 v30, 16, v135
v_and_b32 v31, s15, v135
v_sub_f32 v254, v30, v142
s_mov_b32 s88, 0xd000
buffer_load_dwordx4 v[208:211], v7, s[48:51], s88 offen sc1
v_mfma_f32_32x32x16_bf16 a[224:239], a[108:111], v[124:127], a[224:239]
v_sub_f32 v255, v31, v142
s_waitcnt vmcnt(11)
s_waitcnt lgkmcnt(0)
v_mfma_f32_32x32x16_bf16 a[144:159], a[240:243], v[160:163], a[144:159]
ds_read_b64_tr_b16 a[248:249], v234 offset:24576
ds_read_b64_tr_b16 a[250:251], v234 offset:25600
v_mul_f32 v254, v254, v143
buffer_load_dwordx4 v[212:215], v8, s[48:51], s88 offen sc1
v_mfma_f32_32x32x16_bf16 a[160:175], a[240:243], v[164:167], a[160:175]
ds_read_b64_tr_b16 a[252:253], v9 offset:24576
ds_read_b64_tr_b16 a[254:255], v9 offset:25600
v_mul_f32 v255, v255, v143
v_mfma_f32_32x32x16_bf16 a[176:191], a[244:247], v[160:163], a[176:191]
v_cvt_pk_bf16_f32 v254, v254, v255
v_and_b32 v255, s15, v254
v_lshlrev_b32 v254, 16, v254
v_mul_f32 v46, v254, v148
s_mov_b32 s88, 0xf000
buffer_load_dwordx4 v[216:219], v7, s[48:51], s88 offen sc1
v_mfma_f32_32x32x16_bf16 a[192:207], a[244:247], v[164:167], a[192:207]
v_mul_f32 v47, v255, v149
s_waitcnt vmcnt(11)
s_waitcnt lgkmcnt(0)
v_mfma_f32_32x32x16_bf16 a[144:159], a[248:251], v[168:171], a[144:159]
ds_read_b64_tr_b16 a[240:241], v232 offset:57344
ds_read_b64_tr_b16 a[242:243], v232 offset:58368
buffer_load_dwordx4 v[220:223], v8, s[48:51], s88 offen sc1
v_mfma_f32_32x32x16_bf16 a[160:175], a[248:251], v[172:175], a[160:175]
ds_read_b64_tr_b16 a[244:245], v235 offset:57344
ds_read_b64_tr_b16 a[246:247], v235 offset:58368
v_mfma_f32_32x32x16_bf16 a[176:191], a[252:255], v[168:171], a[176:191]
s_mov_b32 s88, 0x11000
buffer_load_dwordx4 v[80:83], v7, s[48:51], s88 offen sc1
v_mfma_f32_32x32x16_bf16 a[192:207], a[252:255], v[172:175], a[192:207]
s_waitcnt vmcnt(11)
s_waitcnt lgkmcnt(0)
v_mfma_f32_32x32x16_bf16 a[144:159], a[240:243], v[176:179], a[144:159]
ds_read_b64_tr_b16 a[248:249], v233 offset:0
ds_read_b64_tr_b16 a[250:251], v233 offset:1024
buffer_load_dwordx4 v[84:87], v8, s[48:51], s88 offen sc1
v_mfma_f32_32x32x16_bf16 a[160:175], a[240:243], v[180:183], a[160:175]
ds_read_b64_tr_b16 a[252:253], v248 offset:0
ds_read_b64_tr_b16 a[254:255], v248 offset:1024
v_mfma_f32_32x32x16_bf16 a[176:191], a[244:247], v[176:179], a[176:191]
s_mov_b32 s88, 0x13000
buffer_load_dwordx4 v[88:91], v7, s[48:51], s88 offen sc1
v_mfma_f32_32x32x16_bf16 a[192:207], a[244:247], v[180:183], a[192:207]
s_waitcnt vmcnt(11)
s_waitcnt lgkmcnt(0)
v_mfma_f32_32x32x16_bf16 a[144:159], a[248:251], v[184:187], a[144:159]
ds_read_b64_tr_b16 a[240:241], v233 offset:16384
ds_read_b64_tr_b16 a[242:243], v233 offset:17408
buffer_load_dwordx4 v[92:95], v8, s[48:51], s88 offen sc1
v_mfma_f32_32x32x16_bf16 a[160:175], a[248:251], v[188:191], a[160:175]
ds_read_b64_tr_b16 a[244:245], v248 offset:16384
ds_read_b64_tr_b16 a[246:247], v248 offset:17408
ds_write_b128 v156, a[112:115] offset:0
v_mfma_f32_32x32x16_bf16 a[176:191], a[252:255], v[184:187], a[176:191]
ds_write_b128 v157, a[116:119] offset:0
ds_write_b128 v158, a[120:123] offset:0
ds_write_b128 v159, a[124:127] offset:0
s_add_u32 m0, s84, 0x20000
s_mov_b32 s87, 0x142000
buffer_load_dwordx4 v11, s[44:47], s87 offen lds
v_mfma_f32_32x32x16_bf16 a[192:207], a[252:255], v[188:191], a[192:207]
ds_write_b128 v156, a[128:131] offset:16384
buffer_load_dwordx4 v12, s[44:47], s87 offen offset:1024 lds
s_waitcnt vmcnt(12)
s_waitcnt lgkmcnt(5)
v_mfma_f32_32x32x16_bf16 a[144:159], a[240:243], v[192:195], a[144:159]
ds_read_b64_tr_b16 a[248:249], v233 offset:24576
ds_read_b64_tr_b16 a[250:251], v233 offset:25600
s_mov_b32 s88, 0x15000
buffer_load_dwordx4 v[96:99], v7, s[48:51], s88 offen sc1
v_mfma_f32_32x32x16_bf16 a[160:175], a[240:243], v[196:199], a[160:175]
ds_read_b64_tr_b16 a[252:253], v248 offset:24576
ds_read_b64_tr_b16 a[254:255], v248 offset:25600
v_mfma_f32_32x32x16_bf16 a[176:191], a[244:247], v[192:195], a[176:191]
ds_write_b128 v157, a[132:135] offset:16384
ds_write_b128 v158, a[136:139] offset:16384
ds_write_b128 v159, a[140:143] offset:16384
buffer_load_dwordx4 v[100:103], v8, s[48:51], s88 offen sc1
v_mfma_f32_32x32x16_bf16 a[192:207], a[244:247], v[196:199], a[192:207]
ds_write_b128 v156, a[208:211] offset:32768
s_add_u32 m0, s84, 0x1e000
s_mov_b32 s87, 0x15e000
buffer_load_dwordx4 v11, s[44:47], s87 offen lds
s_waitcnt vmcnt(13)
s_waitcnt lgkmcnt(4)
v_mfma_f32_32x32x16_bf16 a[144:159], a[248:251], v[200:203], a[144:159]
ds_read_b64_tr_b16 a[240:241], v233 offset:8192
ds_read_b64_tr_b16 a[242:243], v233 offset:9216
buffer_load_dwordx4 v12, s[44:47], s87 offen offset:1024 lds
v_mfma_f32_32x32x16_bf16 a[160:175], a[248:251], v[204:207], a[160:175]
ds_read_b64_tr_b16 a[244:245], v248 offset:8192
ds_read_b64_tr_b16 a[246:247], v248 offset:9216
ds_write_b128 v157, a[212:215] offset:32768
s_mov_b32 s88, 0x17000
buffer_load_dwordx4 v[104:107], v7, s[48:51], s88 offen sc1
v_mfma_f32_32x32x16_bf16 a[176:191], a[252:255], v[200:203], a[176:191]
ds_write_b128 v158, a[216:219] offset:32768
ds_write_b128 v159, a[220:223] offset:32768
buffer_load_dwordx4 v[108:111], v8, s[48:51], s88 offen sc1
v_mfma_f32_32x32x16_bf16 a[192:207], a[252:255], v[204:207], a[192:207]
ds_write_b128 v156, a[224:227] offset:49152
s_add_u32 m0, s84, 0x24000
s_mov_b32 s87, 0x17a000
buffer_load_dwordx4 v11, s[44:47], s87 offen lds
s_waitcnt vmcnt(15)
s_waitcnt lgkmcnt(4)
v_mfma_f32_32x32x16_bf16 a[144:159], a[240:243], v[208:211], a[144:159]
ds_read_b64_tr_b16 a[248:249], v233 offset:32768
ds_read_b64_tr_b16 a[250:251], v233 offset:33792
buffer_load_dwordx4 v12, s[44:47], s87 offen offset:1024 lds
v_mfma_f32_32x32x16_bf16 a[160:175], a[240:243], v[212:215], a[160:175]
ds_read_b64_tr_b16 a[252:253], v248 offset:32768
ds_read_b64_tr_b16 a[254:255], v248 offset:33792
ds_write_b128 v157, a[228:231] offset:49152
s_mov_b32 s88, 0x19000
buffer_load_dwordx4 v[112:115], v7, s[48:51], s88 offen sc1
v_mfma_f32_32x32x16_bf16 a[176:191], a[244:247], v[208:211], a[176:191]
ds_write_b128 v158, a[232:235] offset:49152
ds_write_b128 v159, a[236:239] offset:49152
buffer_load_dwordx4 v[116:119], v8, s[48:51], s88 offen sc1
v_mfma_f32_32x32x16_bf16 a[192:207], a[244:247], v[212:215], a[192:207]
s_mov_b32 s88, 0x1b000
buffer_load_dwordx4 v[120:123], v7, s[48:51], s88 offen sc1
s_waitcnt vmcnt(17)
s_waitcnt lgkmcnt(3)
v_mfma_f32_32x32x16_bf16 a[144:159], a[248:251], v[216:219], a[144:159]
ds_read_b64_tr_b16 a[240:241], v233 offset:40960
ds_read_b64_tr_b16 a[242:243], v233 offset:41984
s_waitcnt lgkmcnt(2)
s_barrier
buffer_load_dwordx4 v[124:127], v8, s[48:51], s88 offen sc1
v_mfma_f32_32x32x16_bf16 a[160:175], a[248:251], v[220:223], a[160:175]
ds_read_b64_tr_b16 a[244:245], v248 offset:40960
ds_read_b64_tr_b16 a[246:247], v248 offset:41984
ds_read_b128 v[48:51], v13 offset:0
ds_read_b128 v[52:55], v13 offset:2048
ds_read_b128 v[56:59], v13 offset:4096
v_mfma_f32_32x32x16_bf16 a[176:191], a[252:255], v[216:219], a[176:191]
ds_read_b128 v[60:63], v13 offset:6144
ds_read_b128 v[64:67], v13 offset:1024
ds_read_b128 v[68:71], v13 offset:3072
ds_read_b128 v[72:75], v13 offset:5120
ds_read_b128 v[76:79], v13 offset:7168
v_mfma_f32_32x32x16_bf16 a[192:207], a[252:255], v[220:223], a[192:207]
s_waitcnt vmcnt(16)
s_waitcnt lgkmcnt(8)
v_mfma_f32_32x32x16_bf16 a[144:159], a[240:243], v[80:83], a[144:159]
ds_read_b64_tr_b16 a[248:249], v233 offset:49152
ds_read_b64_tr_b16 a[250:251], v233 offset:50176
s_waitcnt lgkmcnt(8)
v_add_f32 v48, v48, v52
v_add_f32 v49, v49, v53
v_add_f32 v50, v50, v54
v_mfma_f32_32x32x16_bf16 a[160:175], a[240:243], v[84:87], a[160:175]
ds_read_b64_tr_b16 a[252:253], v248 offset:49152
ds_read_b64_tr_b16 a[254:255], v248 offset:50176
v_add_f32 v51, v51, v55
s_waitcnt lgkmcnt(8)
v_add_f32 v56, v56, v60
v_add_f32 v57, v57, v61
v_add_f32 v58, v58, v62
v_mfma_f32_32x32x16_bf16 a[176:191], a[244:247], v[80:83], a[176:191]
v_add_f32 v59, v59, v63
v_add_f32 v48, v48, v56
v_add_f32 v49, v49, v57
v_add_f32 v50, v50, v58
v_add_f32 v51, v51, v59
v_fma_f32 v32, 2.0, v48, v32
v_mfma_f32_32x32x16_bf16 a[192:207], a[244:247], v[84:87], a[192:207]
v_fma_f32 v33, 2.0, v49, v33
v_fma_f32 v34, 2.0, v50, v34
v_fma_f32 v35, 2.0, v51, v35
ds_read_b128 v[48:51], v13 offset:8192
s_waitcnt vmcnt(14)
s_waitcnt lgkmcnt(1)
v_mfma_f32_32x32x16_bf16 a[144:159], a[248:251], v[88:91], a[144:159]
ds_read_b64_tr_b16 a[240:241], v234 offset:8192
ds_read_b64_tr_b16 a[242:243], v234 offset:9216
ds_read_b128 v[52:55], v13 offset:10240
ds_read_b128 v[56:59], v13 offset:12288
ds_read_b128 v[60:63], v13 offset:14336
v_mfma_f32_32x32x16_bf16 a[160:175], a[248:251], v[92:95], a[160:175]
ds_read_b64_tr_b16 a[244:245], v9 offset:8192
ds_read_b64_tr_b16 a[246:247], v9 offset:9216
v_add_f32 v64, v64, v68
v_add_f32 v65, v65, v69
v_add_f32 v66, v66, v70
v_add_f32 v67, v67, v71
v_mfma_f32_32x32x16_bf16 a[176:191], a[252:255], v[88:91], a[176:191]
v_add_f32 v72, v72, v76
v_add_f32 v73, v73, v77
v_add_f32 v74, v74, v78
v_add_f32 v75, v75, v79
v_add_f32 v64, v64, v72
v_add_f32 v65, v65, v73
v_mfma_f32_32x32x16_bf16 a[192:207], a[252:255], v[92:95], a[192:207]
v_add_f32 v66, v66, v74
v_add_f32 v67, v67, v75
v_fma_f32 v36, 2.0, v64, v36
v_fma_f32 v37, 2.0, v65, v37
s_waitcnt vmcnt(10)
s_waitcnt lgkmcnt(0)
v_mfma_f32_32x32x16_bf16 a[144:159], a[240:243], v[96:99], a[144:159]
ds_read_b64_tr_b16 a[248:249], v234 offset:0
ds_read_b64_tr_b16 a[250:251], v234 offset:1024
v_fma_f32 v38, 2.0, v66, v38
v_fma_f32 v39, 2.0, v67, v39
ds_read_b128 v[64:67], v13 offset:9216
v_mfma_f32_32x32x16_bf16 a[160:175], a[240:243], v[100:103], a[160:175]
ds_read_b64_tr_b16 a[252:253], v9 offset:0
ds_read_b64_tr_b16 a[254:255], v9 offset:1024
ds_read_b128 v[68:71], v13 offset:11264
ds_read_b128 v[72:75], v13 offset:13312
ds_read_b128 v[76:79], v13 offset:15360
v_mfma_f32_32x32x16_bf16 a[176:191], a[244:247], v[96:99], a[176:191]
v_add_f32 v48, v48, v52
v_add_f32 v49, v49, v53
v_add_f32 v50, v50, v54
v_add_f32 v51, v51, v55
v_add_f32 v56, v56, v60
v_mfma_f32_32x32x16_bf16 a[192:207], a[244:247], v[100:103], a[192:207]
v_add_f32 v57, v57, v61
v_add_f32 v58, v58, v62
s_waitcnt vmcnt(6)
s_waitcnt lgkmcnt(3)
v_mfma_f32_32x32x16_bf16 a[144:159], a[248:251], v[104:107], a[144:159]
ds_read_b64_tr_b16 a[240:241], v233 offset:57344
ds_read_b64_tr_b16 a[242:243], v233 offset:58368
v_add_f32 v59, v59, v63
v_mfma_f32_32x32x16_bf16 a[160:175], a[248:251], v[108:111], a[160:175]
ds_read_b64_tr_b16 a[244:245], v248 offset:57344
ds_read_b64_tr_b16 a[246:247], v248 offset:58368
v_add_f32 v48, v48, v56
v_add_f32 v49, v49, v57
v_mfma_f32_32x32x16_bf16 a[176:191], a[252:255], v[104:107], a[176:191]
v_add_f32 v50, v50, v58
v_add_f32 v51, v51, v59
v_fma_f32 v40, 2.0, v48, v40
v_fma_f32 v41, 2.0, v49, v41
v_mfma_f32_32x32x16_bf16 a[192:207], a[252:255], v[108:111], a[192:207]
v_fma_f32 v42, 2.0, v50, v42
v_fma_f32 v43, 2.0, v51, v43
s_waitcnt vmcnt(2)
s_waitcnt lgkmcnt(0)
v_mfma_f32_32x32x16_bf16 a[144:159], a[240:243], v[112:115], a[144:159]
ds_read_b64_tr_b16 a[248:249], v234 offset:16384
ds_read_b64_tr_b16 a[250:251], v234 offset:17408
v_add_f32 v64, v64, v68
v_mfma_f32_32x32x16_bf16 a[160:175], a[240:243], v[116:119], a[160:175]
ds_read_b64_tr_b16 a[252:253], v9 offset:16384
ds_read_b64_tr_b16 a[254:255], v9 offset:17408
v_add_f32 v65, v65, v69
v_add_f32 v66, v66, v70
v_mfma_f32_32x32x16_bf16 a[176:191], a[244:247], v[112:115], a[176:191]
v_add_f32 v67, v67, v71
v_add_f32 v72, v72, v76
v_add_f32 v73, v73, v77
v_add_f32 v74, v74, v78
v_mfma_f32_32x32x16_bf16 a[192:207], a[244:247], v[116:119], a[192:207]
v_add_f32 v75, v75, v79
v_add_f32 v64, v64, v72
s_waitcnt vmcnt(0)
s_waitcnt lgkmcnt(0)
v_mfma_f32_32x32x16_bf16 a[144:159], a[248:251], v[120:123], a[144:159]
v_add_f32 v65, v65, v73
v_add_f32 v66, v66, v74
v_add_f32 v67, v67, v75
v_fma_f32 v44, 2.0, v64, v44
v_mfma_f32_32x32x16_bf16 a[160:175], a[248:251], v[124:127], a[160:175]
v_fma_f32 v45, 2.0, v65, v45
v_fma_f32 v46, 2.0, v66, v46
v_fma_f32 v47, 2.0, v67, v47
v_add_u32 v156, 0x10000, v156
v_mfma_f32_32x32x16_bf16 a[176:191], a[252:255], v[120:123], a[176:191]
v_add_u32 v157, 0x10000, v157
v_add_u32 v158, 0x10000, v158
v_add_u32 v159, 0x10000, v159
v_add_u32 v13, 0x10000, v13
v_mfma_f32_32x32x16_bf16 a[192:207], a[252:255], v[124:127], a[192:207]
v_mov_b32 v128, 0xbfb8aa3b
v_mov_b32 v129, 0xbfb8aa3b
v_mov_b32 v130, 1.0
v_mov_b32 v131, 1.0
ds_write_b128 v156, a[144:147] offset:0
ds_write_b128 v157, a[148:151] offset:0
ds_write_b128 v158, a[152:155] offset:0
ds_write_b128 v159, a[156:159] offset:0
ds_write_b128 v156, a[160:163] offset:16384
ds_write_b128 v157, a[164:167] offset:16384
ds_write_b128 v158, a[168:171] offset:16384
ds_write_b128 v159, a[172:175] offset:16384
ds_write_b128 v156, a[176:179] offset:32768
ds_write_b128 v157, a[180:183] offset:32768
ds_write_b128 v158, a[184:187] offset:32768
ds_write_b128 v159, a[188:191] offset:32768
ds_write_b128 v156, a[192:195] offset:49152
ds_write_b128 v157, a[196:199] offset:49152
ds_write_b128 v158, a[200:203] offset:49152
ds_write_b128 v159, a[204:207] offset:49152
s_waitcnt lgkmcnt(0)
s_barrier
ds_read_b128 v[48:51], v13 offset:0
ds_read_b128 v[52:55], v13 offset:2048
ds_read_b128 v[56:59], v13 offset:4096
ds_read_b128 v[60:63], v13 offset:6144
ds_read_b128 v[64:67], v13 offset:1024
ds_read_b128 v[68:71], v13 offset:3072
ds_read_b128 v[72:75], v13 offset:5120
ds_read_b128 v[76:79], v13 offset:7168
ds_read_b128 v[80:83], v13 offset:8192
ds_read_b128 v[84:87], v13 offset:10240
ds_read_b128 v[88:91], v13 offset:12288
ds_read_b128 v[92:95], v13 offset:14336
ds_read_b128 v[96:99], v13 offset:9216
ds_read_b128 v[100:103], v13 offset:11264
ds_read_b128 v[104:107], v13 offset:13312
ds_read_b128 v[108:111], v13 offset:15360
s_mov_b32 s11, 0
s_waitcnt lgkmcnt(14)
v_pk_add_f32 v[48:49], v[48:49], v[52:53]
v_pk_add_f32 v[50:51], v[50:51], v[54:55]
s_waitcnt lgkmcnt(12)
v_pk_add_f32 v[56:57], v[56:57], v[60:61]
v_pk_add_f32 v[58:59], v[58:59], v[62:63]
v_pk_add_f32 v[48:49], v[48:49], v[56:57]
v_pk_add_f32 v[50:51], v[50:51], v[58:59]
v_pk_mul_f32 v[52:53], v[48:49], v[128:129]
v_pk_mul_f32 v[54:55], v[50:51], v[128:129]
v_exp_f32 v52, v52
v_exp_f32 v53, v53
v_exp_f32 v54, v54
v_exp_f32 v55, v55
v_pk_mul_f32 v[48:49], v[48:49], v[32:33]
v_pk_mul_f32 v[50:51], v[50:51], v[34:35]
v_pk_add_f32 v[52:53], v[52:53], v[130:131]
v_pk_add_f32 v[54:55], v[54:55], v[130:131]
v_rcp_f32 v52, v52
v_rcp_f32 v53, v53
v_rcp_f32 v54, v54
v_rcp_f32 v55, v55
v_pk_fma_f32 v[48:49], v[48:49], v[52:53], v[16:17]
v_pk_fma_f32 v[50:51], v[50:51], v[54:55], v[18:19]
buffer_store_dwordx4 v[48:51], v151, s[52:55], s11 offen
s_mov_b32 s11, 0x3800
s_waitcnt lgkmcnt(10)
v_pk_add_f32 v[64:65], v[64:65], v[68:69]
v_pk_add_f32 v[66:67], v[66:67], v[70:71]
s_waitcnt lgkmcnt(8)
v_pk_add_f32 v[72:73], v[72:73], v[76:77]
v_pk_add_f32 v[74:75], v[74:75], v[78:79]
v_pk_add_f32 v[64:65], v[64:65], v[72:73]
v_pk_add_f32 v[66:67], v[66:67], v[74:75]
v_pk_mul_f32 v[68:69], v[64:65], v[128:129]
v_pk_mul_f32 v[70:71], v[66:67], v[128:129]
v_exp_f32 v68, v68
v_exp_f32 v69, v69
v_exp_f32 v70, v70
v_exp_f32 v71, v71
v_pk_mul_f32 v[64:65], v[64:65], v[36:37]
v_pk_mul_f32 v[66:67], v[66:67], v[38:39]
v_pk_add_f32 v[68:69], v[68:69], v[130:131]
v_pk_add_f32 v[70:71], v[70:71], v[130:131]
v_rcp_f32 v68, v68
v_rcp_f32 v69, v69
v_rcp_f32 v70, v70
v_rcp_f32 v71, v71
v_pk_fma_f32 v[64:65], v[64:65], v[68:69], v[20:21]
v_pk_fma_f32 v[66:67], v[66:67], v[70:71], v[22:23]
buffer_store_dwordx4 v[64:67], v151, s[52:55], s11 offen
s_mov_b32 s11, 0x7000
s_waitcnt lgkmcnt(6)
v_pk_add_f32 v[80:81], v[80:81], v[84:85]
v_pk_add_f32 v[82:83], v[82:83], v[86:87]
s_waitcnt lgkmcnt(4)
v_pk_add_f32 v[88:89], v[88:89], v[92:93]
v_pk_add_f32 v[90:91], v[90:91], v[94:95]
v_pk_add_f32 v[80:81], v[80:81], v[88:89]
v_pk_add_f32 v[82:83], v[82:83], v[90:91]
v_pk_mul_f32 v[84:85], v[80:81], v[128:129]
v_pk_mul_f32 v[86:87], v[82:83], v[128:129]
v_exp_f32 v84, v84
v_exp_f32 v85, v85
v_exp_f32 v86, v86
v_exp_f32 v87, v87
v_pk_mul_f32 v[80:81], v[80:81], v[40:41]
v_pk_mul_f32 v[82:83], v[82:83], v[42:43]
v_pk_add_f32 v[84:85], v[84:85], v[130:131]
v_pk_add_f32 v[86:87], v[86:87], v[130:131]
v_rcp_f32 v84, v84
v_rcp_f32 v85, v85
v_rcp_f32 v86, v86
v_rcp_f32 v87, v87
v_pk_fma_f32 v[80:81], v[80:81], v[84:85], v[24:25]
v_pk_fma_f32 v[82:83], v[82:83], v[86:87], v[26:27]
buffer_store_dwordx4 v[80:83], v151, s[52:55], s11 offen
s_mov_b32 s11, 0xa800
s_waitcnt lgkmcnt(2)
v_pk_add_f32 v[96:97], v[96:97], v[100:101]
v_pk_add_f32 v[98:99], v[98:99], v[102:103]
s_waitcnt lgkmcnt(0)
v_pk_add_f32 v[104:105], v[104:105], v[108:109]
v_pk_add_f32 v[106:107], v[106:107], v[110:111]
v_pk_add_f32 v[96:97], v[96:97], v[104:105]
v_pk_add_f32 v[98:99], v[98:99], v[106:107]
v_pk_mul_f32 v[100:101], v[96:97], v[128:129]
v_pk_mul_f32 v[102:103], v[98:99], v[128:129]
v_exp_f32 v100, v100
v_exp_f32 v101, v101
v_exp_f32 v102, v102
v_exp_f32 v103, v103
v_pk_mul_f32 v[96:97], v[96:97], v[44:45]
v_pk_mul_f32 v[98:99], v[98:99], v[46:47]
v_pk_add_f32 v[100:101], v[100:101], v[130:131]
v_pk_add_f32 v[102:103], v[102:103], v[130:131]
v_rcp_f32 v100, v100
v_rcp_f32 v101, v101
v_rcp_f32 v102, v102
v_rcp_f32 v103, v103
v_pk_fma_f32 v[96:97], v[96:97], v[100:101], v[28:29]
v_pk_fma_f32 v[98:99], v[98:99], v[102:103], v[30:31]
buffer_store_dwordx4 v[96:99], v151, s[52:55], s11 offen
s_endpgm
.La_retry:
s_mov_b32 s90, 0x20000
.La_loop:
s_mov_b32 s91, 0
buffer_load_dwordx2 v[250:251], v243, s[56:59], s91 offen sc1
s_waitcnt vmcnt(0)
v_cmp_ne_u64 vcc, v[250:251], v[246:247]
s_nop 1
s_cbranch_vccz .La_ok
s_sub_u32 s90, s90, 1
s_cmp_lg_u32 s90, 0
s_cbranch_scc1 .La_loop
s_branch .La_ok
.Lh_retry:
s_mov_b32 s90, 0x20000
.Lh_loop:
s_mov_b32 s91, 0x20000
buffer_load_dwordx2 v[250:251], v243, s[56:59], s91 offen sc1
s_waitcnt vmcnt(0)
v_cmp_ne_u64 vcc, v[250:251], v[246:247]
s_nop 1
s_cbranch_vccz .Lh_ok
s_sub_u32 s90, s90, 1
s_cmp_lg_u32 s90, 0
s_cbranch_scc1 .Lh_loop
s_branch .Lh_ok
.Lb_retry:
s_mov_b32 s90, 0x20000
.Lb_loop:
s_mov_b32 s91, 0x10000
buffer_load_dwordx2 v[250:251], v243, s[56:59], s91 offen sc1
s_waitcnt vmcnt(0)
v_cmp_ne_u64 vcc, v[250:251], v[246:247]
s_nop 1
s_cbranch_vccz .Lb_ok
s_sub_u32 s90, s90, 1
s_cmp_lg_u32 s90, 0
s_cbranch_scc1 .Lb_loop
s_branch .Lb_ok
.Lg_retry:
s_mov_b32 s90, 0x20000
.Lg_loop:
buffer_load_dwordx4 v[128:131], v241, s[80:83], 0 offen sc1
buffer_load_dwordx4 v[132:135], v241, s[80:83], 0 offen offset:1024 sc1
s_waitcnt vmcnt(0)
v_add_u32 v252, v129, v131
v_add3_u32 v252, v252, v133, v135
s_waitcnt vmcnt(0)
v_cmp_ne_u32 vcc, v252, v248
s_nop 1
s_cbranch_vccz .Lg_ok
s_sub_u32 s90, s90, 1
s_cmp_lg_u32 s90, 0
s_cbranch_scc1 .Lg_loop
s_branch .Lg_ok
.Lhelper:
s_sub_u32 s10, s10, 28
s_lshl_b32 s10, s10, 3
s_add_u32 s10, s10, s11
s_lshl_b32 s10, s10, 6
v_lshrrev_b32 v1, 6, %2
v_and_b32 v2, 63, %2
v_lshl_add_u32 v1, v1, 11, v2
v_add_u32 v1, s10, v1
v_lshlrev_b32 v2, 8, v1
v_lshlrev_b32 v1, 7, v1
v_add_u32 v3, 0x100000, v1
s_waitcnt lgkmcnt(0)
buffer_load_dword v8, v1, s[20:23], 0 offen
buffer_load_dword v9, v1, s[24:27], 0 offen
buffer_load_dword v10, v2, s[28:31], 0 offen
buffer_load_dword v11, v2, s[32:35], 0 offen
buffer_load_dword v12, v1, s[36:39], 0 offen
buffer_load_dword v13, v2, s[28:31], 0 offen offset:128
buffer_load_dword v14, v2, s[32:35], 0 offen offset:128
buffer_load_dword v15, v1, s[40:43], 0 offen
buffer_load_dword v16, v1, s[44:47], 0 offen
buffer_load_dword v17, v3, s[44:47], 0 offen
s_endpgm
.fill 64, 4, 0xBF800000
.Lk_end:
)ASM" :: "s"(__builtin_amdgcn_kernarg_segment_ptr()), "s"(blockIdx.x), "v"(threadIdx.x) : "v1", "v2", "v3", "v4", "v5", "v6", "v7", "v8", "v9", "v10", "v11", "v12", "v13", "v14", "v15", "v16", "v17", "v18", "v19", "v20", "v21", "v22", "v23", "v24", "v25", "v26", "v27", "v28", "v29", "v30", "v31", "v32", "v33", "v34", "v35", "v36", "v37", "v38", "v39", "v40", "v41", "v42", "v43", "v44", "v45", "v46", "v47", "v48", "v49", "v50", "v51", "v52", "v53", "v54", "v55", "v56", "v57", "v58", "v59", "v60", "v61", "v62", "v63", "v64", "v65", "v66", "v67", "v68", "v69", "v70", "v71", "v72", "v73", "v74", "v75", "v76", "v77", "v78", "v79", "v80", "v81", "v82", "v83", "v84", "v85", "v86", "v87", "v88", "v89", "v90", "v91", "v92", "v93", "v94", "v95", "v96", "v97", "v98", "v99", "v100", "v101", "v102", "v103", "v104", "v105", "v106", "v107", "v108", "v109", "v110", "v111", "v112", "v113", "v114", "v115", "v116", "v117", "v118", "v119", "v120", "v121", "v122", "v123", "v124", "v125", "v126", "v127", "v128", "v129", "v130", "v131", "v132", "v133", "v134", "v135", "v136", "v137", "v138", "v139", "v140", "v141", "v142", "v143", "v144", "v145", "v146", "v147", "v148", "v149", "v150", "v151", "v152", "v153", "v154", "v155", "v156", "v157", "v158", "v159", "v160", "v161", "v162", "v163", "v164", "v165", "v166", "v167", "v168", "v169", "v170", "v171", "v172", "v173", "v174", "v175", "v176", "v177", "v178", "v179", "v180", "v181", "v182", "v183", "v184", "v185", "v186", "v187", "v188", "v189", "v190", "v191", "v192", "v193", "v194", "v195", "v196", "v197", "v198", "v199", "v200", "v201", "v202", "v203", "v204", "v205", "v206", "v207", "v208", "v209", "v210", "v211", "v212", "v213", "v214", "v215", "v216", "v217", "v218", "v219", "v220", "v221", "v222", "v223", "v224", "v225", "v226", "v227", "v228", "v229", "v230", "v231", "v232", "v233", "v234", "v235", "v236", "v237", "v238", "v239", "v240", "v241", "v242", "v243", "v244", "v245", "v246", "v247", "v248", "v249", "v250", "v251", "v252", "v253", "v254", "v255", "a0", "a1", "a2", "a3", "a4", "a5", "a6", "a7", "a8", "a9", "a10", "a11", "a12", "a13", "a14", "a15", "a16", "a17", "a18", "a19", "a20", "a21", "a22", "a23", "a24", "a25", "a26", "a27", "a28", "a29", "a30", "a31", "a32", "a33", "a34", "a35", "a36", "a37", "a38", "a39", "a40", "a41", "a42", "a43", "a44", "a45", "a46", "a47", "a48", "a49", "a50", "a51", "a52", "a53", "a54", "a55", "a56", "a57", "a58", "a59", "a60", "a61", "a62", "a63", "a64", "a65", "a66", "a67", "a68", "a69", "a70", "a71", "a72", "a73", "a74", "a75", "a76", "a77", "a78", "a79", "a80", "a81", "a82", "a83", "a84", "a85", "a86", "a87", "a88", "a89", "a90", "a91", "a92", "a93", "a94", "a95", "a96", "a97", "a98", "a99", "a100", "a101", "a102", "a103", "a104", "a105", "a106", "a107", "a108", "a109", "a110", "a111", "a112", "a113", "a114", "a115", "a116", "a117", "a118", "a119", "a120", "a121", "a122", "a123", "a124", "a125", "a126", "a127", "a128", "a129", "a130", "a131", "a132", "a133", "a134", "a135", "a136", "a137", "a138", "a139", "a140", "a141", "a142", "a143", "a144", "a145", "a146", "a147", "a148", "a149", "a150", "a151", "a152", "a153", "a154", "a155", "a156", "a157", "a158", "a159", "a160", "a161", "a162", "a163", "a164", "a165", "a166", "a167", "a168", "a169", "a170", "a171", "a172", "a173", "a174", "a175", "a176", "a177", "a178", "a179", "a180", "a181", "a182", "a183", "a184", "a185", "a186", "a187", "a188", "a189", "a190", "a191", "a192", "a193", "a194", "a195", "a196", "a197", "a198", "a199", "a200", "a201", "a202", "a203", "a204", "a205", "a206", "a207", "a208", "a209", "a210", "a211", "a212", "a213", "a214", "a215", "a216", "a217", "a218", "a219", "a220", "a221", "a222", "a223", "a224", "a225", "a226", "a227", "a228", "a229", "a230", "a231", "a232", "a233", "a234", "a235", "a236", "a237", "a238", "a239", "a240", "a241", "a242", "a243", "a244", "a245", "a246", "a247", "a248", "a249", "a250", "a251", "a252", "a253", "a254", "a255", "s3", "s4", "s5", "s6", "s7", "s8", "s9", "s10", "s11", "s12", "s13", "s14", "s15", "s16", "s17", "s18", "s19", "s20", "s21", "s22", "s23", "s24", "s25", "s26", "s27", "s28", "s29", "s30", "s31", "s32", "s33", "s34", "s35", "s36", "s37", "s38", "s39", "s40", "s41", "s42", "s43", "s44", "s45", "s46", "s47", "s48", "s49", "s50", "s51", "s52", "s53", "s54", "s55", "s56", "s57", "s58", "s59", "s60", "s61", "s62", "s63", "s64", "s65", "s66", "s67", "s68", "s69", "s70", "s71", "s72", "s73", "s74", "s75", "s76", "s77", "s78", "s79", "s80", "s81", "s82", "s83", "s84", "s85", "s86", "s87", "s88", "s89", "s90", "s91", "s92", "s93", "s94", "s95", "vcc", "scc", "memory");
}

extern "C" void kernel_launch(void* const* d_in, const int* in_sizes, int n_in, void* d_out, int out_size, void* d_ws, size_t ws_size, hipStream_t stream) {
    char* ws = (char*)d_ws;
    const uint32_t fl = 0x00020000u, sq = 401408u, wide = 1605632u;
    Args a = {{
        {d_in[0], 1835008u, fl}, {d_in[3], sq, fl}, {d_in[4], sq, fl}, {d_in[10], wide, fl},
        {d_in[11], wide, fl}, {d_in[5], sq, fl}, {d_in[6], sq, fl}, {d_in[12], wide, fl},
        {ws, 11010048u, fl}, {d_out, 3670016u, fl}, {ws + (12u << 20), 573440u, fl}},
        {d_in[1], d_in[2], d_in[7], d_in[8]}, {ws + (13u << 20), 229376u, fl}};
    fk<<<256, 256, 0, stream>>>(a);
}
```
